```python
import torch

def _softmax(x, axis=-1):
    return torch.softmax(x, dim=axis)
def _gelu(x):
    return torch.nn.functional.gelu(x, approximate="tanh")
def _silu(x):
    return torch.nn.functional.silu(x)
def _sigmoid(x):
    return torch.sigmoid(x)
def _layernorm(x, axis=-1, eps=1e-5):
    m = x.mean(dim=axis, keepdim=True)
    v = x.var(dim=axis, keepdim=True, unbiased=False)
    return (x - m) / torch.sqrt(v + eps)
def _scatter_add(tgt, idx, src, axis=0):
    return tgt.clone().index_add_(axis, idx, src)
def _segment_sum(src, seg, n):
    out = torch.zeros((n,) + src.shape[1:], dtype=src.dtype)
    return out.index_add_(0, seg, src)
def _pad(x, pw):
    flat = tuple(v for pair in reversed(pw) for v in pair)
    return torch.nn.functional.pad(x, flat)

kernel_name = 'concat_heads__spectral_gfnet__ssm_conv_h3__prompt'

def setup_inputs():

    _w = (lambda a: (lambda t: t.to(torch.bfloat16) if t.is_floating_point() else t)(torch.as_tensor(a)))
    def _A_setup():
        B, S, D = 1, 2048, 640
        Df = D // 2 + 1
        return {
            "x": _w((lambda *s: torch.randn(*s, dtype=torch.bfloat16) * 0.05)(B, S, D)),
            "w_r": _w((lambda *s: torch.randn(*s, dtype=torch.bfloat16) * 0.05)(S, Df) * 0.02),
            "w_i": _w((lambda *s: torch.randn(*s, dtype=torch.bfloat16) * 0.05)(S, Df) * 0.02),
        }
    _A_in = _A_setup()

    def _B_setup():
        B, S, D = 1, 2048, 640
        ks = 4
        return {
            "q": _w((lambda *s: torch.randn(*s, dtype=torch.bfloat16) * 0.05)(B, S, D)),
            "k": _w((lambda *s: torch.randn(*s, dtype=torch.bfloat16) * 0.1)(B, S, D)),
            "v": _w((lambda *s: torch.randn(*s, dtype=torch.bfloat16) * 0.1)(B, S, D)),
            "shift_k": _w((lambda *s: torch.randn(*s, dtype=torch.bfloat16) * 0.1)(D, ks) * 0.5),
            "ssm_k": _w((lambda *s: torch.randn(*s, dtype=torch.bfloat16) * 0.1)(D, S) * (1.0 / S ** 0.5)),
            "ssm_D": _w((lambda *s: torch.randn(*s, dtype=torch.bfloat16) * 0.1)(D)),
        }
    _B_in = _B_setup()

    return {
        "A_x": _A_in["x"],
        "A_w_r": _A_in["w_r"],
        "A_w_i": _A_in["w_i"],
        "B_q": _B_in["q"],
        "B_k": _B_in["k"],
        "B_v": _B_in["v"],
        "B_shift_k": _B_in["shift_k"],
        "B_ssm_k": _B_in["ssm_k"],
        "B_ssm_D": _B_in["ssm_D"],
    }

def reference(A_x, A_w_r, A_w_i, B_q, B_k, B_v, B_shift_k, B_ssm_k, B_ssm_D):
    def _A_ref(x, w_r, w_i):
        B, S, D = x.shape
        X = (lambda x, axes=(-2,-1): torch.fft.rfft2(x.float(), dim=axes))(x, axes=(-2, -1))
        W = w_r + 1j * w_i
        X = X * W
        y = (lambda x, s, axes=(-2,-1): torch.fft.irfft2(x, s=s, dim=axes).to(torch.bfloat16))(X, (S, D), axes=(-2, -1))
        return y

    def _B_ref(q, k, v, shift_k, ssm_k, ssm_D):
        B, S, D = q.shape
        ks = shift_k.shape[-1]
        k_pad = _pad(k, ((0, 0), (ks - 1, 0), (0, 0)))
        k_shift = torch.zeros_like(k)
        for t in range(ks):
            k_shift = k_shift + k_pad[:, t:t + S, :] * shift_k[:, ks - 1 - t][None, None, :]
        kv = k_shift * v
        L = 2 * S
        kv_pad = _pad(kv, ((0, 0), (0, S), (0, 0)))
        kf_pad = _pad(ssm_k, ((0, 0), (0, S)))
        kv_f = (lambda x, axes=(-2,-1): torch.fft.rfft2(x.float(), dim=axes))(kv_pad[:, None, :, :], axes=(1, 2))
        kf_f = (lambda x, axes=(-2,-1): torch.fft.rfft2(x.float(), dim=axes))(kf_pad[:, None, :], axes=(1, 2))
        kf_f = (lambda x, *d: x.permute(*d))(kf_f, 1, 2, 0)
        y_f = kv_f * kf_f[None, :, :, :]
        y = (lambda x, s, axes=(-2,-1): torch.fft.irfft2(x, s=s, dim=axes).to(torch.bfloat16))(y_f, (1, L), axes=(1, 2))[:, 0, :S, :]
        y = y + kv * ssm_D[None, None, :]
        return q * y

    a = _A_ref(A_x, A_w_r, A_w_i)
    b = _B_ref(B_q, B_k, B_v, B_shift_k, B_ssm_k, B_ssm_D)
    return torch.concatenate([a, b], axis=-1)
```

```cpp
#include <hip/hip_runtime.h>
#include <hip/hip_bf16.h>

typedef __attribute__((ext_vector_type(4))) short s16x4;
typedef __attribute__((ext_vector_type(8))) short s16x8;
typedef __attribute__((ext_vector_type(2))) __bf16 bf16x2;
typedef __attribute__((ext_vector_type(8))) __bf16 bf16x8;
typedef __attribute__((ext_vector_type(2))) float f32x2;
typedef __attribute__((ext_vector_type(4))) float f32x4;
typedef __attribute__((ext_vector_type(16))) float f32x16;
typedef __attribute__((ext_vector_type(2))) unsigned u32x2;
typedef __attribute__((ext_vector_type(4))) unsigned u32x4;
typedef __attribute__((address_space(3))) void* lds_vp;
typedef __attribute__((address_space(3))) s16x4* lds_s16x4p;
typedef u32x2 __attribute__((aligned(4))) u32x2_a4;

#define DI __device__ __forceinline__

constexpr int S_ = 2048, D_ = 640, DF = 321;
constexpr int NID = 16 * DF;
constexpr int XP = 5152;

DI unsigned pk(float lo, float hi) {
    f32x2 v = {lo, hi};
    return __builtin_bit_cast(unsigned, __builtin_convertvector(v, bf16x2));
}
DI float bflo(unsigned u) { return __builtin_bit_cast(float, u << 16); }
DI float bfhi(unsigned u) { return __builtin_bit_cast(float, u & 0xffff0000u); }
DI f32x16 mfma(u32x4 a, u32x4 b, f32x16 c) {
    return __builtin_amdgcn_mfma_f32_32x32x16_bf16(__builtin_bit_cast(bf16x8, a), __builtin_bit_cast(bf16x8, b), c, 0, 0, 0);
}
DI float fsin(float rev) { return __builtin_amdgcn_sinf(rev); }
DI float fcos(float rev) { return __builtin_amdgcn_cosf(rev); }
DI unsigned cs(float rev) { return pk(fcos(rev), fsin(rev)); }
DI u32x4 trread2(const char* p0, const char* p1) {
    s16x4 a = __builtin_amdgcn_ds_read_tr16_b64_v4i16((lds_s16x4p)p0);
    s16x4 b = __builtin_amdgcn_ds_read_tr16_b64_v4i16((lds_s16x4p)p1);
    return __builtin_bit_cast(u32x4, __builtin_shufflevector(a, b, 0, 1, 2, 3, 4, 5, 6, 7));
}
DI void dma16(const void* g, char* l) { __builtin_amdgcn_global_load_lds(g, (lds_vp)l, 16, 0, 0); }
DI u32x4 pack8(const f32x16& x, int o) {
    u32x4 r = {pk(x[o], x[o + 1]), pk(x[o + 2], x[o + 3]), pk(x[o + 4], x[o + 5]), pk(x[o + 6], x[o + 7])};
    return r;
}
DI f32x16 zero16() { f32x16 z = {0.f, 0.f, 0.f, 0.f, 0.f, 0.f, 0.f, 0.f, 0.f, 0.f, 0.f, 0.f, 0.f, 0.f, 0.f, 0.f}; return z; }

constexpr int P1_XS = 0;
constexpr int P1_EX = 20480;
constexpr int P1_MT = P1_EX + 336 * 80;
constexpr int P1_LDS = P1_MT + 14 * 1024;

__global__ __launch_bounds__(512) void k_p1(const unsigned short* __restrict__ x, const unsigned short* __restrict__ kk,
                                            const unsigned short* __restrict__ vv, const unsigned short* __restrict__ shk,
                                            unsigned* __restrict__ X1, unsigned short* __restrict__ kvt) {
    __shared__ __attribute__((aligned(16))) char smem[P1_LDS];
    const int tid = threadIdx.x, lane = tid & 63;
    const int w = __builtin_amdgcn_readfirstlane(tid >> 6);
    const int n = lane & 31, hh = lane >> 5;
    if (blockIdx.x < 128) {
        const int i2 = blockIdx.x;
        char* xs = smem + P1_XS + w * 2560;
        {
            const char* r0 = (const char*)(x + (size_t)(128 * (2 * w) + i2) * D_);
            const char* r1 = r0 + 128 * D_ * 2;
            dma16(r0 + lane * 16, xs);
            dma16(r1 + lane * 16, xs + 1024);
            if (lane < 32) dma16((lane < 16 ? r0 : r1) + 1024 + (lane & 15) * 16, xs + 2048);
        }
        char* mt = smem + P1_MT;
        if (w < 3) {
            float c[8], s[8];
#pragma unroll
            for (int j = 0; j < 8; ++j) {
                int d1 = 16 * w + 8 * hh + j;
                float a = (float)((d1 * n) % 40) * (1.0f / 40.0f);
                bool ok = d1 < 40;
                c[j] = ok ? fcos(a) : 0.f;
                s[j] = ok ? -fsin(a) : 0.f;
            }
            u32x4 fr = {pk(c[0], c[1]), pk(c[2], c[3]), pk(c[4], c[5]), pk(c[6], c[7])};
            u32x4 fi = {pk(s[0], s[1]), pk(s[2], s[3]), pk(s[4], s[5]), pk(s[6], s[7])};
            *(u32x4*)(mt + (2 * w) * 1024 + lane * 16) = fr;
            *(u32x4*)(mt + (2 * w + 1) * 1024 + lane * 16) = fi;
        } else if (w == 3) {
#pragma unroll
            for (int j2 = 0; j2 < 4; ++j2) {
                int j = 2 * j2;
                int d2 = (j & 3) + 8 * (j >> 2) + 4 * hh;
                float a0 = (float)(d2 * n) * (1.0f / 640.0f), a1 = (float)((d2 + 1) * n) * (1.0f / 640.0f);
                f32x4 t = {fcos(a0), fsin(a0), fcos(a1), fsin(a1)};
                *(f32x4*)(mt + (10 + j2) * 1024 + lane * 16) = t;
            }
        } else if (w == 4) {
            const int k2 = n >> 1, ri = n & 1;
            const float ph = ri ? (k2 >= 8 ? -0.25f : 0.25f) : 0.f;
            float c[8], s[8];
#pragma unroll
            for (int j = 0; j < 8; ++j) {
                int d2 = (j & 3) + 8 * (j >> 2) + 4 * hh;
                float a = (float)(d2 * k2) * (1.0f / 16.0f) + ph;
                c[j] = fcos(a);
                s[j] = fsin(a);
            }
            u32x4 fr = {pk(c[0], c[1]), pk(c[2], c[3]), pk(c[4], c[5]), pk(c[6], c[7])};
            u32x4 fi = {pk(s[0], s[1]), pk(s[2], s[3]), pk(s[4], s[5]), pk(s[6], s[7])};
            *(u32x4*)(mt + 6 * 1024 + lane * 16) = fr;
            *(u32x4*)(mt + 7 * 1024 + lane * 16) = fi;
        } else if (w == 5) {
            const int k1s = n >> 1, ri = n & 1;
#pragma unroll
            for (int sg = 0; sg < 2; ++sg) {
                u32x4 f;
#pragma unroll
                for (int t = 0; t < 4; ++t) {
                    int i1 = 8 * sg + 4 * hh + t;
                    f[t] = cs((float)((128 * i1 + i2) * k1s) * (1.0f / 2048.0f) + 0.25f * ri);
                }
                *(u32x4*)(mt + (8 + sg) * 1024 + lane * 16) = f;
            }
        }
        __syncthreads();
        const int g = lane >> 4, q = (lane >> 2) & 3, p = lane & 3;
        const char* xa = xs + (g & 1) * 1024 + (8 * hh + q) * 32 + p * 8;
        const char* xt = xs + 2048 + (g & 1) * 256 + q * 32 + p * 8;
        u32x4 a0 = trread2(xa, xa + 128), a1 = trread2(xa + 512, xa + 640), a2 = trread2(xt, xt + 128);
        f32x16 tr = zero16(), ti = zero16();
        tr = mfma(a0, *(const u32x4*)(mt + 0 * 1024 + lane * 16), tr);
        ti = mfma(a0, *(const u32x4*)(mt + 1 * 1024 + lane * 16), ti);
        tr = mfma(a1, *(const u32x4*)(mt + 2 * 1024 + lane * 16), tr);
        ti = mfma(a1, *(const u32x4*)(mt + 3 * 1024 + lane * 16), ti);
        tr = mfma(a2, *(const u32x4*)(mt + 4 * 1024 + lane * 16), tr);
        ti = mfma(a2, *(const u32x4*)(mt + 5 * 1024 + lane * 16), ti);
#pragma unroll
        for (int j2 = 0; j2 < 4; ++j2) {
            f32x4 t = *(const f32x4*)(mt + (10 + j2) * 1024 + lane * 16);
#pragma unroll
            for (int e = 0; e < 2; ++e)
#pragma unroll
                for (int ss = 0; ss < 2; ++ss) {
                    int r = 8 * ss + 2 * j2 + e;
                    float c = t[2 * e], s = t[2 * e + 1], re = tr[r], im = ti[r];
                    tr[r] = re * c + im * s;
                    ti[r] = im * c - re * s;
                }
        }
        const u32x4 m2r = *(const u32x4*)(mt + 6 * 1024 + lane * 16), m2i = *(const u32x4*)(mt + 7 * 1024 + lane * 16);
        f32x16 y0 = mfma(m2r, pack8(tr, 0), zero16());
        f32x16 y1 = mfma(m2r, pack8(tr, 8), zero16());
        y0 = mfma(m2i, pack8(ti, 0), y0);
        y1 = mfma(m2i, pack8(ti, 8), y1);
        char* ex = smem + P1_EX;
        if (n <= 20) {
#pragma unroll
            for (int t = 0; t < 8; ++t) {
                int k2 = (t & 1) + 4 * (t >> 1) + 2 * hh;
                u32x2 v = {pk(y0[2 * t], y0[2 * t + 1]), pk(y1[2 * t], y1[2 * t + 1])};
                *(u32x2*)(ex + (21 * k2 + n) * 80 + 8 * w) = v;
            }
        }
        __syncthreads();
        if (w < 6) {
            const u32x4 c0 = *(const u32x4*)(mt + 8 * 1024 + lane * 16), c1 = *(const u32x4*)(mt + 9 * 1024 + lane * 16);
            f32x16 o[2];
#pragma unroll
            for (int e = 0; e < 2; ++e) {
                int kd = min(64 * w + 2 * n + e, 320);
                int m = kd / 40, c = kd - 40 * m;
                int slot = (c <= 20) ? 21 * m + c : 21 * (15 - m) + 40 - c;
                const char* src = ex + slot * 80 + 16 * hh;
                o[e] = mfma(c0, *(const u32x4*)src, zero16());
                o[e] = mfma(c1, *(const u32x4*)(src + 32), o[e]);
            }
            unsigned* dst = X1 + (size_t)i2 * XP + 2 * hh * DF + 64 * w + 2 * n;
            if (w < 5) {
#pragma unroll
                for (int t = 0; t < 8; ++t) {
                    u32x2 v = {pk(o[0][2 * t], o[0][2 * t + 1]), pk(o[1][2 * t], o[1][2 * t + 1])};
                    *(u32x2_a4*)(dst + ((t & 1) + 4 * (t >> 1)) * DF) = v;
                }
            } else if (n == 0) {
#pragma unroll
                for (int t = 0; t < 8; ++t) dst[((t & 1) + 4 * (t >> 1)) * DF] = pk(o[0][2 * t], o[0][2 * t + 1]);
            }
        }
    } else {
        const int b = blockIdx.x - 128;
        const int cg = b % 10, sb = b / 10;
        const int cp = tid & 31, rg = tid >> 5;
        const int ch = 64 * cg + 2 * cp, s0 = 128 * sb + 8 * rg;
        const unsigned* kp = (const unsigned*)(kk + (size_t)s0 * D_ + ch);
        const unsigned* vp = (const unsigned*)(vv + (size_t)s0 * D_ + ch);
        unsigned kr[11], vr[8];
#pragma unroll
        for (int i = 0; i < 3; ++i) kr[i] = (s0 > 0) ? kp[(i - 3) * (D_ / 2)] : 0u;
#pragma unroll
        for (int i = 3; i < 11; ++i) kr[i] = kp[(i - 3) * (D_ / 2)];
#pragma unroll
        for (int i = 0; i < 8; ++i) vr[i] = vp[i * (D_ / 2)];
        const u32x4 sw = *(const u32x4*)(shk + ch * 4);
        f32x2 wt[4] = {{bflo(sw[0]), bflo(sw[2])}, {bfhi(sw[0]), bfhi(sw[2])}, {bflo(sw[1]), bflo(sw[3])}, {bfhi(sw[1]), bfhi(sw[3])}};
        f32x2 kf[11], o[8];
#pragma unroll
        for (int i = 0; i < 11; ++i) kf[i] = f32x2{bflo(kr[i]), bfhi(kr[i])};
#pragma unroll
        for (int e = 0; e < 8; ++e) {
            f32x2 a = kf[e] * wt[3];
            a = kf[e + 1] * wt[2] + a;
            a = kf[e + 2] * wt[1] + a;
            a = kf[e + 3] * wt[0] + a;
            o[e] = a * f32x2{bflo(vr[e]), bfhi(vr[e])};
        }
#pragma unroll
        for (int c = 0; c < 2; ++c) {
            u32x4 u = {pk(o[0][c], o[1][c]), pk(o[2][c], o[3][c]), pk(o[4][c], o[5][c]), pk(o[6][c], o[7][c])};
            *(u32x4*)(smem + (c * 32 + cp) * 272 + rg * 16) = u;
        }
        __syncthreads();
        const int g = lane >> 4, q = (lane >> 2) & 3, p = lane & 3;
#pragma unroll
        for (int it = 0; it < 2; ++it) {
            int row = w * 8 + 2 * g + it;
            const char* src = smem + row * 272 + 64 * p + 8 * (7 - q);
            u32x4 u = trread2(src, src - 32);
            int c = 64 * cg + 2 * (row & 31) + (row >> 5);
            *(u32x4*)(kvt + (size_t)c * S_ + 128 * sb + (lane & 15) * 8) = u;
        }
    }
}

constexpr int P2_H = 64, P2_U = 4608, P2_C = 8704;
constexpr int P2_LDS = 32768;

template <int CTRL, int BANK>
DI float dppo(float old, float v) {
    return __builtin_bit_cast(float, __builtin_amdgcn_update_dpp(__builtin_bit_cast(int, old), __builtin_bit_cast(int, v), CTRL, 0xf, BANK, false));
}
DI float reduce_tile(f32x16& a) {
#pragma unroll
    for (int u = 0; u < 8; ++u) {
        float t = dppo<0x114, 0xa>(0.f, a[2 * u + 1]);
        if (u > 0) t = dppo<0x104, 0x5>(t, a[2 * u - 1]);
        a[2 * u] += t;
    }
    float c = dppo<0x104, 0x5>(0.f, a[15]) + dppo<0x128, 0x3>(0.f, a[14]);
#pragma unroll
    for (int i = 0; i < 4; ++i) {
        float t = dppo<0x128, 0xc>(0.f, a[4 * i + 2]);
        if (i > 0) t = dppo<0x128, 0x3>(t, a[4 * i - 2]);
        a[4 * i] += t;
    }
    return c;
}

__global__ __launch_bounds__(256, 4) void k_p2(const unsigned* __restrict__ X1, const unsigned short* __restrict__ wr,
                                               const unsigned short* __restrict__ wi, unsigned* __restrict__ Z1,
                                               const unsigned short* __restrict__ kvt, const unsigned short* __restrict__ hk,
                                               const unsigned short* __restrict__ dd, unsigned short* __restrict__ yt) {
    __shared__ __attribute__((aligned(16))) char smem[P2_LDS];
    const int tid = threadIdx.x, lane = tid & 63;
    const int w = __builtin_amdgcn_readfirstlane(tid >> 6);
    const int n = lane & 31, hh = lane >> 5;
    if (blockIdx.x < 640) {
        const int ch = blockIdx.x;
        dma16((const char*)(hk + (size_t)ch * S_) + tid * 16, smem + P2_H + w * 1024);
        dma16((const char*)(kvt + (size_t)ch * S_) + tid * 16, smem + P2_U + w * 1024);
        const float dv = bflo(dd[ch]);
        if (tid < 33) {
            u32x4 z = {0u, 0u, 0u, 0u};
            *(u32x4*)(smem + (tid < 4 ? tid * 16 : (tid < 32 ? 4160 + (tid - 4) * 16 : P2_C))) = z;
        }
        __syncthreads();
        const int g = lane >> 4, q = (lane >> 2) & 3, p = lane & 3;
        const int pi = 2 * (g & 1) + (p >> 1) + 4 * (p & 1);
        const int a = (n & 3) | ((n >> 4) << 2), r = (n >> 2) & 3;
        const int T1 = 7 - w, T2 = w;
        const char* pa = smem + 8 + 8 * (8 * hh + q + pi);
        const char* pb = smem + P2_U + 64 * (a - hh) + 16 * r;
        const char* pb1 = pb + 512 * T1 - 384;
        const char* pb2 = pb + 512 * T2 - 384;
        f32x16 acc1 = zero16(), acc2 = zero16();
        for (int c = 0; c <= T2; ++c) {
#pragma unroll
            for (int ks = 0; ks < 4; ++ks) {
                u32x4 fa = trread2(pa + 128 * ks, pa + 128 * ks + 32);
                acc1 = mfma(fa, *(const u32x4*)(pb1 + 128 * (3 - ks)), acc1);
                acc2 = mfma(fa, *(const u32x4*)(pb2 + 128 * (3 - ks)), acc2);
            }
            pa += 512; pb1 -= 512; pb2 -= 512;
        }
        for (int c = T2 + 1; c <= T1; ++c) {
#pragma unroll
            for (int ks = 0; ks < 4; ++ks) {
                u32x4 fa = trread2(pa + 128 * ks, pa + 128 * ks + 32);
                acc1 = mfma(fa, *(const u32x4*)(pb1 + 128 * (3 - ks)), acc1);
            }
            pa += 512; pb1 -= 512;
        }
        float c2 = reduce_tile(acc2);
        float c1 = reduce_tile(acc1);
        float* cb = (float*)(smem + P2_C) + (2 * a + hh) * 4 + r;
        cb[64 * T1 + 4] = c1;
        cb[64 * T2 + 4] = c2;
        const char* pu = smem + P2_U + 64 * a + 16 * r + (hh ? 0 : 8);
        u32x2 u1 = *(const u32x2*)(pu + 512 * T1), u2 = *(const u32x2*)(pu + 512 * T2);
        __syncthreads();
        acc1[0] += cb[64 * T1];
        acc2[0] += cb[64 * T2];
        u32x2 o1 = {pk(acc1[0] + dv * bfhi(u1[1]), acc1[4] + dv * bflo(u1[1])), pk(acc1[8] + dv * bfhi(u1[0]), acc1[12] + dv * bflo(u1[0]))};
        u32x2 o2 = {pk(acc2[0] + dv * bfhi(u2[1]), acc2[4] + dv * bflo(u2[1])), pk(acc2[8] + dv * bfhi(u2[0]), acc2[12] + dv * bflo(u2[0]))};
        unsigned short* yo = yt + (size_t)ch * S_ + lane * 4;
        *(u32x2*)(yo + 256 * T1) = o1;
        *(u32x2*)(yo + 256 * T2) = o2;
    } else {
        const int id = 32 * (blockIdx.x - 640) + n;
        u32x4 xb[4];
        const unsigned* xp = X1 + (size_t)(64 * hh + 4 * w) * XP + id;
#pragma unroll
        for (int e = 0; e < 4; ++e)
#pragma unroll
            for (int t = 0; t < 4; ++t) xb[e][t] = xp[(16 * t + e) * XP];
        const int idw = min(id, NID - 1);
        float wre[2][8], wim[2][8];
#pragma unroll
        for (int e = 0; e < 2; ++e)
#pragma unroll
            for (int t = 0; t < 8; ++t) {
                int kb = (t & 1) + 4 * (t >> 1) + 2 * hh;
                int off = (2 * w + e + 8 * kb) * NID + idw;
                wre[e][t] = bflo(wr[off]);
                wim[e][t] = bflo(wi[off]);
            }
        const int ri = n & 1;
        const float ph = 0.25f * ri;
        u32x4 fa[2], fi[2];
        {
            const int kr = n >> 2, sel = (n >> 1) & 1;
#pragma unroll
            for (int t = 0; t < 4; ++t) {
                float an = (float)((4 * hh + t) * kr) * 0.125f;
                unsigned f = cs(ph + an), b = cs(ph - an);
                fa[0][t] = sel ? 0u : f; fa[1][t] = sel ? f : 0u;
                fi[0][t] = sel ? 0u : b; fi[1][t] = sel ? b : 0u;
            }
        }
        u32x4 mb[2][2], mi[2][2];
#pragma unroll
        for (int e = 0; e < 2; ++e)
#pragma unroll
            for (int sg = 0; sg < 2; ++sg)
#pragma unroll
                for (int t = 0; t < 4; ++t) {
                    int ka = 2 * w + e;
                    int ib = 8 * sg + 4 * hh + t;
                    mb[e][sg][t] = cs(ph + (float)(ib * (ka + 8 * (n >> 1))) * (1.0f / 128.0f));
                    int kb = 8 * sg + (t & 1) + 4 * (t >> 1) + 2 * hh;
                    mi[e][sg][t] = cs(ph - (float)((n >> 1) * (ka + 8 * kb)) * (1.0f / 128.0f));
                }
        f32x16 d0 = mfma(fa[0], xb[0], zero16());
        f32x16 d1 = mfma(fa[0], xb[2], zero16());
        d0 = mfma(fa[1], xb[1], d0);
        d1 = mfma(fa[1], xb[3], d1);
#pragma unroll
        for (int gq = 0; gq < 4; ++gq) {
            u32x4 v = {pk(d0[4 * gq], d0[4 * gq + 1]), pk(d0[4 * gq + 2], d0[4 * gq + 3]), pk(d1[4 * gq], d1[4 * gq + 1]), pk(d1[4 * gq + 2], d1[4 * gq + 3])};
            *(u32x4*)(smem + (((2 * gq + hh) * 4 + w) * 32 + n) * 16) = v;
        }
        __syncthreads();
        f32x16 v2[2];
#pragma unroll
        for (int e = 0; e < 2; ++e) {
            const char* src = smem + (((2 * w + e) * 4 + hh) * 32 + n) * 16;
            f32x16 y = mfma(mb[e][0], *(const u32x4*)src, zero16());
            y = mfma(mb[e][1], *(const u32x4*)(src + 1024), y);
#pragma unroll
            for (int t = 0; t < 8; ++t) {
                float re = y[2 * t], im = y[2 * t + 1];
                y[2 * t] = re * wre[e][t] - im * wim[e][t];
                y[2 * t + 1] = re * wim[e][t] + im * wre[e][t];
            }
            v2[e] = mfma(mi[e][0], pack8(y, 0), zero16());
            v2[e] = mfma(mi[e][1], pack8(y, 8), v2[e]);
        }
        char* e2 = smem + 16384;
#pragma unroll
        for (int t = 0; t < 8; ++t) {
            int ib = (t & 1) + 4 * (t >> 1) + 2 * hh;
            u32x2 v = {pk(v2[0][2 * t], v2[0][2 * t + 1]), pk(v2[1][2 * t], v2[1][2 * t + 1])};
            *(u32x2*)(e2 + ((ib * 2 + (w >> 1)) * 32 + n) * 16 + (w & 1) * 8) = v;
        }
        __syncthreads();
        const char* src = e2 + ((4 * w * 2 + hh) * 32 + n) * 16;
        d0 = mfma(fi[0], *(const u32x4*)src, zero16());
        d1 = mfma(fi[0], *(const u32x4*)(src + 2048), zero16());
        d0 = mfma(fi[1], *(const u32x4*)(src + 1024), d0);
        d1 = mfma(fi[1], *(const u32x4*)(src + 3072), d1);
        unsigned* zp = Z1 + (size_t)(16 * hh + 4 * w) * XP + id;
#pragma unroll
        for (int gq = 0; gq < 4; ++gq) {
            zp[(32 * gq + 0) * XP] = pk(d0[4 * gq], d0[4 * gq + 1]);
            zp[(32 * gq + 1) * XP] = pk(d0[4 * gq + 2], d0[4 * gq + 3]);
            zp[(32 * gq + 2) * XP] = pk(d1[4 * gq], d1[4 * gq + 1]);
            zp[(32 * gq + 3) * XP] = pk(d1[4 * gq + 2], d1[4 * gq + 3]);
        }
    }
}

constexpr int P3_EX = 0;
constexpr int P3_MT = 336 * 80;
constexpr int P3_LDS = P3_MT + 16 * 1024;

__global__ __launch_bounds__(512) void k_p3(const unsigned* __restrict__ Z1, const unsigned short* __restrict__ yt,
                                            const unsigned short* __restrict__ qq, unsigned short* __restrict__ out) {
    __shared__ __attribute__((aligned(16))) char smem[P3_LDS];
    const int tid = threadIdx.x, lane = tid & 63;
    const int w = __builtin_amdgcn_readfirstlane(tid >> 6);
    const int n = lane & 31, hh = lane >> 5;
    if (blockIdx.x < 128) {
        const int i2 = blockIdx.x;
        u32x4 zb[2][2];
        int exo[2];
#pragma unroll
        for (int e = 0; e < 2; ++e) {
            int c = min(32 * (w + 8 * e) + n, 335);
            int k2 = c / 21, k1 = c - 21 * k2;
            int kd = k2 < 8 ? k1 + 40 * k2 : 40 * (16 - k2) - k1;
            exo[e] = (2 * hh * 21 + k1) * 80 + 4 * k2;
            const unsigned* zp = Z1 + (size_t)i2 * XP + 4 * hh * DF + kd;
            if (e == 0 || w < 3) {
#pragma unroll
                for (int sg = 0; sg < 2; ++sg)
#pragma unroll
                    for (int t = 0; t < 4; ++t) zb[e][sg][t] = zp[(8 * sg + t) * DF];
            }
        }
        char* mt = smem + P3_MT;
        if (w == 3) {
            const int i1 = n >> 1, ri = n & 1;
#pragma unroll
            for (int sg = 0; sg < 2; ++sg) {
                u32x4 f;
#pragma unroll
                for (int t = 0; t < 4; ++t) {
                    int k1s = 8 * sg + 4 * hh + t;
                    f[t] = cs(0.25f * ri - (float)((128 * i1 + i2) * k1s) * (1.0f / 2048.0f));
                }
                *(u32x4*)(mt + sg * 1024 + lane * 16) = f;
            }
        } else if (w == 4) {
            const int e = n & 15, ro = n >> 4;
            const int d2 = (e & 3) + 8 * ((e >> 2) & 1) + 4 * (e >> 3);
#pragma unroll
            for (int sg = 0; sg < 2; ++sg) {
                u32x4 f;
#pragma unroll
                for (int t = 0; t < 4; ++t) {
                    int k2 = 8 * sg + 4 * hh + t;
                    float an = 0.25f * ro - (float)(d2 * k2) * (1.0f / 16.0f);
                    f[t] = cs(sg ? -an : an);
                }
                *(u32x4*)(mt + (2 + sg) * 1024 + lane * 16) = f;
            }
        } else if (w == 5) {
            const int e = n & 15;
            const int d2 = (e & 3) + 8 * ((e >> 2) & 1) + 4 * (e >> 3);
#pragma unroll
            for (int j2 = 0; j2 < 6; ++j2) {
                int j = 2 * j2;
                int k1 = (j & 3) + 8 * (j >> 2) + 4 * hh;
                float a0 = (float)(d2 * k1) * (1.0f / 640.0f), a1 = (float)(d2 * (k1 + 1)) * (1.0f / 640.0f);
                f32x4 t = {fcos(a0), fsin(a0), fcos(a1), fsin(a1)};
                *(f32x4*)(mt + (10 + j2) * 1024 + lane * 16) = t;
            }
        } else if (w >= 6) {
            const int d1 = 32 * (w - 6) + n;
            float c[12], s[12];
#pragma unroll
            for (int j = 0; j < 12; ++j) {
                int k1 = (j & 3) + 8 * (j >> 2) + 4 * hh;
                float a = (float)((d1 * k1) % 40) * (1.0f / 40.0f);
                float wt = (k1 > 20) ? 0.f : ((k1 == 0 || k1 == 20) ? 1.0f : 2.0f) * (1.0f / (2048.0f * 640.0f));
                c[j] = wt * fcos(a);
                s[j] = -wt * fsin(a);
            }
            u32x4 f0 = {pk(c[0], c[1]), pk(c[2], c[3]), pk(c[4], c[5]), pk(c[6], c[7])};
            u32x4 f1 = {pk(s[0], s[1]), pk(s[2], s[3]), pk(s[4], s[5]), pk(s[6], s[7])};
            u32x4 f2 = {pk(c[8], c[9]), pk(c[10], c[11]), pk(s[8], s[9]), pk(s[10], s[11])};
            char* dst = mt + (4 + 3 * (w - 6)) * 1024 + lane * 16;
            *(u32x4*)dst = f0;
            *(u32x4*)(dst + 1024) = f1;
            *(u32x4*)(dst + 2048) = f2;
        }
        __syncthreads();
        char* ex = smem + P3_EX;
        {
            const u32x4 c0 = *(const u32x4*)(mt + lane * 16), c1 = *(const u32x4*)(mt + 1024 + lane * 16);
#pragma unroll
            for (int e = 0; e < 2; ++e) {
                if (e == 0 || w < 3) {
                    f32x16 o = mfma(c0, zb[e][0], zero16());
                    o = mfma(c1, zb[e][1], o);
#pragma unroll
                    for (int t = 0; t < 8; ++t)
                        *(unsigned*)(ex + exo[e] + ((t & 1) + 4 * (t >> 1)) * 21 * 80) = pk(o[2 * t], o[2 * t + 1]);
                }
            }
        }
        __syncthreads();
        const char* src = ex + (2 * w * 21 + min(n, 20)) * 80 + 16 * hh;
        const u32x4 m0 = *(const u32x4*)(mt + 2 * 1024 + lane * 16), m1 = *(const u32x4*)(mt + 3 * 1024 + lane * 16);
        f32x16 tr = mfma(*(const u32x4*)src, m0, zero16());
        f32x16 ti = mfma(*(const u32x4*)(src + 21 * 80), m0, zero16());
        tr = mfma(*(const u32x4*)(src + 32), m1, tr);
        ti = mfma(*(const u32x4*)(src + 21 * 80 + 32), m1, ti);
#pragma unroll
        for (int j2 = 0; j2 < 6; ++j2) {
            f32x4 t = *(const f32x4*)(mt + (10 + j2) * 1024 + lane * 16);
#pragma unroll
            for (int e = 0; e < 2; ++e) {
                int r = 2 * j2 + e;
                auto sw = __builtin_amdgcn_permlane16_swap(__float_as_uint(tr[r]), __float_as_uint(ti[r]), false, false);
                float re = __uint_as_float(sw[0]), im = __uint_as_float(sw[1]);
                float c = t[2 * e], s = t[2 * e + 1];
                tr[r] = re * c - im * s;
                ti[r] = im * c + re * s;
            }
        }
        const u32x4 a0 = pack8(tr, 0), a1 = pack8(ti, 0);
        const u32x4 a2 = {pk(tr[8], tr[9]), pk(tr[10], tr[11]), pk(ti[8], ti[9]), pk(ti[10], ti[11])};
        unsigned short* op = out + (size_t)(128 * 2 * w + i2) * (2 * D_) + 16 * n + 8 * hh;
#pragma unroll
        for (int tl = 0; tl < 2; ++tl) {
            const char* mp = mt + (4 + 3 * tl) * 1024 + lane * 16;
            f32x16 y = mfma(a0, *(const u32x4*)mp, zero16());
            y = mfma(a1, *(const u32x4*)(mp + 1024), y);
            y = mfma(a2, *(const u32x4*)(mp + 2048), y);
            if (tl == 0 || n < 8) {
                *(u32x4*)(op + 512 * tl) = pack8(y, 0);
                *(u32x4*)(op + 512 * tl + 128 * 2 * D_) = pack8(y, 8);
            }
        }
    } else {
        const int b = blockIdx.x - 128;
        const int cg = b % 10, pb = b / 10;
#pragma unroll
        for (int it = 0; it < 2; ++it) {
            int c = it * 32 + w * 4 + (lane >> 4);
            int un = (lane & 15) ^ ((c >> 1) & 15);
            dma16(yt + (size_t)(64 * cg + c) * S_ + 128 * pb + un * 8, smem + it * 8192 + w * 1024);
        }
        const int cp = tid & 31, U = tid >> 5;
        const int a = ((2 * U) & 3) | ((U >> 3) << 2), r = (U >> 1) & 3;
        const int s0 = 256 * (pb >> 1) + 16 * (pb & 1) + 32 * a + r;
        const unsigned* qp = (const unsigned*)(qq + (size_t)s0 * D_ + 64 * cg + 2 * cp);
        unsigned qr[8];
#pragma unroll
        for (int j = 0; j < 8; ++j) qr[j] = qp[(32 * (j >> 2) + 4 * (j & 3)) * (D_ / 2)];
        __syncthreads();
        const char* src = smem + cp * 512 + ((U ^ (cp & 15)) * 16);
        const u32x4 y0 = *(const u32x4*)src, y1 = *(const u32x4*)(src + 256);
        unsigned* op = (unsigned*)(out + (size_t)s0 * (2 * D_) + D_ + 64 * cg + 2 * cp);
#pragma unroll
        for (int j = 0; j < 8; ++j) {
            float f0 = (j & 1) ? bfhi(y0[j >> 1]) : bflo(y0[j >> 1]);
            float f1 = (j & 1) ? bfhi(y1[j >> 1]) : bflo(y1[j >> 1]);
            op[(32 * (j >> 2) + 4 * (j & 3)) * D_] = pk(bflo(qr[j]) * f0, bfhi(qr[j]) * f1);
        }
    }
}

extern "C" void kernel_launch(void* const* d_in, const int* in_sizes, int n_in, void* d_out, int out_size, void* d_ws,
                              size_t ws_size, hipStream_t stream) {
    const unsigned short* x = (const unsigned short*)d_in[0];
    const unsigned short* wr = (const unsigned short*)d_in[1];
    const unsigned short* wi = (const unsigned short*)d_in[2];
    const unsigned short* q = (const unsigned short*)d_in[3];
    const unsigned short* k = (const unsigned short*)d_in[4];
    const unsigned short* v = (const unsigned short*)d_in[5];
    const unsigned short* shk = (const unsigned short*)d_in[6];
    const unsigned short* hk = (const unsigned short*)d_in[7];
    const unsigned short* dd = (const unsigned short*)d_in[8];
    char* ws = (char*)d_ws;
    unsigned* X1 = (unsigned*)ws;
    unsigned* Z1 = (unsigned*)(ws + (4u << 20));
    unsigned short* kvt = (unsigned short*)(ws + (8u << 20));
    unsigned short* yt = (unsigned short*)(ws + (12u << 20));
    k_p1<<<128 + 160, 512, 0, stream>>>(x, k, v, shk, X1, kvt);
    k_p2<<<640 + 161, 256, 0, stream>>>(X1, wr, wi, Z1, kvt, hk, dd, yt);
    k_p3<<<128 + 160, 512, 0, stream>>>(Z1, yt, q, (unsigned short*)d_out);
}
```

```cpp
#define W_EARLY
#define CODE_WGS 0
#include <hip/hip_runtime.h>
#include <hip/hip_bf16.h>

typedef __attribute__((ext_vector_type(4))) short s16x4;
typedef __attribute__((ext_vector_type(2))) __bf16 bf16x2;
typedef __attribute__((ext_vector_type(8))) __bf16 bf16x8;
typedef __attribute__((ext_vector_type(2))) float f32x2;
typedef __attribute__((ext_vector_type(4))) float f32x4;
typedef __attribute__((ext_vector_type(16))) float f32x16;
typedef __attribute__((ext_vector_type(2))) unsigned u32x2;
typedef __attribute__((ext_vector_type(4))) unsigned u32x4;
typedef __attribute__((address_space(3))) s16x4* lds_s16x4p;
typedef __attribute__((address_space(3))) void* lds_vp;
typedef unsigned short u16;
typedef __amdgpu_buffer_rsrc_t rsrc_t;

#define DI __device__ __forceinline__

#define STAMP(I)
#define STAMP_DECL
#define STAMP_OUT
#define COUNT(I)

namespace tab {
constexpr double PI = 3.14159265358979323846;
constexpr double csin(double x) { double t = x, s = x; for (int i = 1; i < 12; ++i) { t *= -x * x / ((2 * i) * (2 * i + 1)); s += t; } return s; }
constexpr double ccos(double x) { double t = 1, s = 1; for (int i = 1; i < 12; ++i) { t *= -x * x / ((2 * i - 1) * (2 * i)); s += t; } return s; }
struct Base { float c[640]; };
constexpr Base mkbase() {
    Base b{};
    for (int i = 0; i < 640; ++i) {
        int m = i > 320 ? 640 - i : i;
        b.c[i] = m <= 160 ? (m <= 80 ? (float)ccos(2 * PI * m / 640) : (float)csin(2 * PI * (160 - m) / 640))
                          : (m >= 240 ? (float)-ccos(2 * PI * (320 - m) / 640) : (float)-csin(2 * PI * (m - 160) / 640));
    }
    return b;
}
constexpr Base base = mkbase();
constexpr float C(int n) { return base.c[((n % 640) + 640) % 640]; }
constexpr float S(int n) { return C(n - 160); }
constexpr unsigned fb(float f) { return __builtin_bit_cast(unsigned, f); }
constexpr unsigned bf(float f) { unsigned u = fb(f); return (u + 0x7fffu + ((u >> 16) & 1u)) >> 16; }
constexpr unsigned p2(float lo, float hi) { return bf(lo) | (bf(hi) << 16); }
struct T1 { unsigned v[12 * 256]; };
struct T3 { unsigned v[14 * 256]; };
constexpr T1 mk1() {
    T1 t{};
    for (int l = 0; l < 64; ++l) {
        const int n = l & 31, hh = l >> 5;
        for (int k = 0; k < 4; ++k) {
            for (int sg = 0; sg < 3; ++sg) {
                const int d1 = 16 * sg + 8 * hh + 2 * k;
                const bool ok = d1 < 40;
                t.v[((2 * sg) * 64 + l) * 4 + k] = ok ? p2(C(16 * d1 * n), C(16 * (d1 + 1) * n)) : 0u;
                t.v[((2 * sg + 1) * 64 + l) * 4 + k] = ok ? p2(-S(16 * d1 * n), -S(16 * (d1 + 1) * n)) : 0u;
            }
            const int k2 = n >> 1, ph = (n & 1) ? (k2 >= 8 ? -160 : 160) : 0;
            const int d2 = 2 * (k & 1) + 8 * (k >> 1) + 4 * hh;
            t.v[(6 * 64 + l) * 4 + k] = p2(C(40 * d2 * k2 + ph), C(40 * (d2 + 1) * k2 + ph));
            t.v[(7 * 64 + l) * 4 + k] = p2(S(40 * d2 * k2 + ph), S(40 * (d2 + 1) * k2 + ph));
            for (int e = 0; e < 2; ++e) {
                t.v[((8 + e) * 64 + l) * 4 + k] = fb(C((k + 8 * e + 4 * hh) * n));
                t.v[((10 + e) * 64 + l) * 4 + k] = fb(S((k + 8 * e + 4 * hh) * n));
            }
        }
    }
    return t;
}
constexpr float wt(int k1) { return k1 > 20 ? 0.f : ((k1 == 0 || k1 == 20) ? 1.0f : 2.0f) * (1.0f / (2048.0f * 640.0f)); }
constexpr T3 mk3() {
    T3 t{};
    for (int l = 0; l < 64; ++l) {
        const int n = l & 31, hh = l >> 5, e = n & 15, ro = n >> 4;
        const int d2 = (e & 3) + 8 * ((e >> 2) & 1) + 4 * (e >> 3);
        for (int k = 0; k < 4; ++k) {
            const int a0 = 160 * ro - 40 * d2 * (4 * hh + k), a1 = 40 * d2 * (8 + 4 * hh + k) - 160 * ro;
            t.v[(0 * 64 + l) * 4 + k] = p2(C(a0), S(a0));
            t.v[(1 * 64 + l) * 4 + k] = p2(C(a1), S(a1));
            for (int tl = 0; tl < 2; ++tl) {
                const int d1 = 32 * tl + n;
                const int ka = 2 * (k & 1) + 8 * (k >> 1) + 4 * hh, kb = 16 + 2 * (k & 1) + 4 * hh;
                t.v[((2 + 3 * tl) * 64 + l) * 4 + k] = p2(wt(ka) * C(16 * d1 * ka), wt(ka + 1) * C(16 * d1 * (ka + 1)));
                t.v[((3 + 3 * tl) * 64 + l) * 4 + k] = p2(-wt(ka) * S(16 * d1 * ka), -wt(ka + 1) * S(16 * d1 * (ka + 1)));
                t.v[((4 + 3 * tl) * 64 + l) * 4 + k] = k < 2 ? p2(wt(kb) * C(16 * d1 * kb), wt(kb + 1) * C(16 * d1 * (kb + 1)))
                                                             : p2(-wt(kb) * S(16 * d1 * kb), -wt(kb + 1) * S(16 * d1 * (kb + 1)));
            }
            for (int j = 0; j < 3; ++j) {
                t.v[((8 + j) * 64 + l) * 4 + k] = fb(C(d2 * (k + 8 * j + 4 * hh)));
                t.v[((11 + j) * 64 + l) * 4 + k] = fb(S(d2 * (k + 8 * j + 4 * hh)));
            }
        }
    }
    return t;
}
}
__device__ __attribute__((aligned(128))) const tab::T1 g_t1 = tab::mk1();
__device__ __attribute__((aligned(128))) const tab::T3 g_t3 = tab::mk3();

constexpr int S_ = 2048, D_ = 640, DF = 321;
constexpr int NW = 8 * DF;
constexpr int PK = 108;
constexpr int ROWB = 8 * PK * 16;
constexpr int MSGB = 1088;
constexpr unsigned WS_X1 = 0, WS_Z1 = 4u << 20, WS_KV = 8u << 20, WS_EP = 12u << 20, WS_END = 13u << 20;
constexpr unsigned SPIN_CAP = 1u << 17;
constexpr int CHB = 8832, LB_H = 64, LB_Y = 2112, LB_Z = 4160, LB_U = 4672, LB_ST = 8 * CHB, LDS_BYTES = LB_ST + 80 * 136;

DI unsigned pk(float lo, float hi) {
    f32x2 v = {lo, hi};
    return __builtin_bit_cast(unsigned, __builtin_convertvector(v, bf16x2));
}
DI float bflo(unsigned u) { return __uint_as_float(u << 16); }
DI float bfhi(unsigned u) { return __uint_as_float(u & 0xffff0000u); }
DI f32x16 mfma(u32x4 a, u32x4 b, f32x16 c) {
    return __builtin_amdgcn_mfma_f32_32x32x16_bf16(__builtin_bit_cast(bf16x8, a), __builtin_bit_cast(bf16x8, b), c, 0, 0, 0);
}
DI f32x4 mfma16(u32x4 a, u32x4 b, f32x4 c) {
    return __builtin_amdgcn_mfma_f32_16x16x32_bf16(__builtin_bit_cast(bf16x8, a), __builtin_bit_cast(bf16x8, b), c, 0, 0, 0);
}
DI float fsin(float rev) { return __builtin_amdgcn_sinf(rev); }
DI float fcos(float rev) { return __builtin_amdgcn_cosf(rev); }
DI unsigned cs(float rev) { return pk(fcos(rev), fsin(rev)); }
DI u32x4 trread2(const char* p0, const char* p1) {
    s16x4 a = __builtin_amdgcn_ds_read_tr16_b64_v4i16((lds_s16x4p)p0);
    s16x4 b = __builtin_amdgcn_ds_read_tr16_b64_v4i16((lds_s16x4p)p1);
    return __builtin_bit_cast(u32x4, __builtin_shufflevector(a, b, 0, 1, 2, 3, 4, 5, 6, 7));
}
DI u32x4 pack8(const f32x16& x, int o) {
    u32x4 r = {pk(x[o], x[o + 1]), pk(x[o + 2], x[o + 3]), pk(x[o + 4], x[o + 5]), pk(x[o + 6], x[o + 7])};
    return r;
}
DI f32x16 zero16() { f32x16 z = {0.f, 0.f, 0.f, 0.f, 0.f, 0.f, 0.f, 0.f, 0.f, 0.f, 0.f, 0.f, 0.f, 0.f, 0.f, 0.f}; return z; }
DI f32x4 zero4() { f32x4 z = {0.f, 0.f, 0.f, 0.f}; return z; }
#define PIN(x) asm volatile("" : "+v"(x))
#define DRAIN() asm volatile("s_waitcnt vmcnt(0)" ::: "memory")
#define BAR() do { asm volatile("s_waitcnt lgkmcnt(0)" ::: "memory"); __builtin_amdgcn_s_barrier(); asm volatile("" ::: "memory"); } while (0)
#define ST_AUX 16
#define LD_AUX 16
#define LDB_AUX 16
#define DLY_H 10
DI void st16(rsrc_t rs, unsigned off, unsigned so, u32x4 v) { __builtin_amdgcn_raw_buffer_store_b128(v, rs, off, so, ST_AUX); }
DI u32x4 ld16(rsrc_t rs, unsigned off, unsigned so) { return __builtin_amdgcn_raw_buffer_load_b128(rs, off, so, LD_AUX); }
DI u32x4 ld16b(rsrc_t rs, unsigned off, unsigned so) { return __builtin_amdgcn_raw_buffer_load_b128(rs, off, so, LDB_AUX); }
DI unsigned ld4(rsrc_t rs, unsigned off, unsigned so) { return __builtin_amdgcn_raw_buffer_load_b32(rs, off, so, 16); }
DI void st4(rsrc_t rs, unsigned off, unsigned so, unsigned v) { __builtin_amdgcn_raw_buffer_store_b32(v, rs, off, so, 16); }
DI unsigned xor3(unsigned a, unsigned b, unsigned c) { return __builtin_amdgcn_bitop3_b32(a, b, c, 0x96); }
DI u32x4 piece(unsigned a, unsigned b, unsigned c, unsigned tag) { u32x4 v = {a, b, c, xor3(a, b, c) ^ tag}; return v; }
DI f32x4 asf(u32x4 v) { return __builtin_bit_cast(f32x4, v); }
DI f32x4 sub4(const f32x16& x, int o) { f32x4 r = {x[o], x[o + 1], x[o + 2], x[o + 3]}; return r; }
DI u32x4 pk44(f32x4 a, f32x4 b) { u32x4 r = {pk(a[0], a[1]), pk(a[2], a[3]), pk(b[0], b[1]), pk(b[2], b[3])}; return r; }
DI u32x4 pkc(f32x4 re, f32x4 im) { u32x4 r = {pk(re[0], im[0]), pk(re[1], im[1]), pk(re[2], im[2]), pk(re[3], im[3])}; return r; }
DI rsrc_t mkrs(const void* p, unsigned bytes) { return __builtin_amdgcn_make_buffer_rsrc((void*)p, 0, bytes, 0x00020000); }
DI void dma16(const void* g, unsigned lds) {
    unsigned keep;
    asm volatile("s_mov_b32 %0, m0\n\ts_mov_b32 m0, %2\n\ts_nop 0\n\tglobal_load_lds_dwordx4 %1, off\n\ts_mov_b32 m0, %0" : "=&s"(keep) : "v"(g), "s"(lds) : "memory");
}

DI void fetch4(rsrc_t rs, unsigned off, unsigned stride, unsigned tag, u32x4 (&f)[3]) {
    u32x4 x[4];
#pragma unroll 1
    for (unsigned spins = 0; spins < SPIN_CAP; ++spins) {
        asm volatile("" ::: "memory");
        unsigned c[4];
#pragma unroll
        for (int t = 0; t < 4; ++t) x[t] = ld16(rs, off, t * stride);
#pragma unroll
        for (int t = 0; t < 4; ++t) c[t] = xor3(xor3(x[t][0], x[t][1], x[t][2]), x[t][3], tag);
        if (__builtin_amdgcn_ballot_w64((c[0] | c[1] | c[2] | c[3]) != 0) == 0) break;
    }
#pragma unroll
    for (int e = 0; e < 3; ++e) f[e] = u32x4{x[0][e], x[1][e], x[2][e], x[3][e]};
}

template <int CTRL, int BANK>
DI float dppo(float old, float v) {
    return __int_as_float(__builtin_amdgcn_update_dpp(__float_as_int(old), __float_as_int(v), CTRL, 0xf, BANK, false));
}
template <int CTRL>
DI float dppz(float v) { return __int_as_float(__builtin_amdgcn_update_dpp(0, __float_as_int(v), CTRL, 0xf, 0xf, true)); }
DI void reduce_tile(const f32x16& a, f32x4& o, float& c) {
    float r0 = a[0], r1 = a[1], r2 = a[2], r3 = a[3], r4 = a[4], r5 = a[5], r6 = a[6], r7 = a[7];
    float r8 = a[8], r9 = a[9], r10 = a[10], r11 = a[11], r12 = a[12], r13 = a[13], r14 = a[14], r15 = a[15];
    asm volatile(
        "s_nop 7\n\t"
        "s_nop 3\n\t"
        "v_mov_b32 %16, 0\n\t"
        "v_add_f32_dpp %0, %1, %0 row_shr:4 row_mask:0xf bank_mask:0xa\n\t"
        "v_add_f32_dpp %2, %3, %2 row_shr:4 row_mask:0xf bank_mask:0xa\n\t"
        "v_add_f32_dpp %4, %5, %4 row_shr:4 row_mask:0xf bank_mask:0xa\n\t"
        "v_add_f32_dpp %6, %7, %6 row_shr:4 row_mask:0xf bank_mask:0xa\n\t"
        "v_add_f32_dpp %8, %9, %8 row_shr:4 row_mask:0xf bank_mask:0xa\n\t"
        "v_add_f32_dpp %10, %11, %10 row_shr:4 row_mask:0xf bank_mask:0xa\n\t"
        "v_add_f32_dpp %12, %13, %12 row_shr:4 row_mask:0xf bank_mask:0xa\n\t"
        "v_add_f32_dpp %14, %15, %14 row_shr:4 row_mask:0xf bank_mask:0xa\n\t"
        "v_mov_b32_dpp %16, %15 row_shl:4 row_mask:0xf bank_mask:0x5\n\t"
        "v_add_f32_dpp %2, %1, %2 row_shl:4 row_mask:0xf bank_mask:0x5\n\t"
        "v_add_f32_dpp %6, %5, %6 row_shl:4 row_mask:0xf bank_mask:0x5\n\t"
        "v_add_f32_dpp %10, %9, %10 row_shl:4 row_mask:0xf bank_mask:0x5\n\t"
        "v_add_f32_dpp %14, %13, %14 row_shl:4 row_mask:0xf bank_mask:0x5\n\t"
        "v_add_f32_dpp %4, %3, %4 row_shl:4 row_mask:0xf bank_mask:0x5\n\t"
        "v_add_f32_dpp %8, %7, %8 row_shl:4 row_mask:0xf bank_mask:0x5\n\t"
        "v_add_f32_dpp %12, %11, %12 row_shl:4 row_mask:0xf bank_mask:0x5\n\t"
        "v_add_f32_dpp %0, %2, %0 row_ror:8 row_mask:0xf bank_mask:0xc\n\t"
        "v_add_f32_dpp %4, %6, %4 row_ror:8 row_mask:0xf bank_mask:0xc\n\t"
        "v_add_f32_dpp %8, %10, %8 row_ror:8 row_mask:0xf bank_mask:0xc\n\t"
        "v_add_f32_dpp %12, %14, %12 row_ror:8 row_mask:0xf bank_mask:0xc\n\t"
        "v_add_f32_dpp %16, %14, %16 row_ror:8 row_mask:0xf bank_mask:0x3\n\t"
        "v_add_f32_dpp %4, %2, %4 row_ror:8 row_mask:0xf bank_mask:0x3\n\t"
        "v_add_f32_dpp %8, %6, %8 row_ror:8 row_mask:0xf bank_mask:0x3\n\t"
        "v_add_f32_dpp %12, %10, %12 row_ror:8 row_mask:0xf bank_mask:0x3\n\t"
        : "+v"(r0), "+v"(r1), "+v"(r2), "+v"(r3), "+v"(r4), "+v"(r5), "+v"(r6), "+v"(r7), "+v"(r8), "+v"(r9), "+v"(r10), "+v"(r11),
          "+v"(r12), "+v"(r13), "+v"(r14), "+v"(r15), "=&v"(c));
    o = f32x4{r0, r4, r8, r12};
}

#define CODE_BYTES 21248
#define OUT_AUX 0

template <int V> struct IC { static constexpr int v = V; };
struct ConvCtx {
    int pa, pb, bz, pu, csrc, yo;
    float dv;
    bool first, diag;
};
DI float edge_sum(f32x4 d, bool diag) {
    float t = dppz<0x104>(d[3]) + dppz<0x108>(d[2]) + dppz<0x10c>(d[1]);
    t = diag ? t : 0.f;
    t = __builtin_amdgcn_mfma_f32_16x16x4f32(1.0f, t, zero4(), 0, 0, 0)[0];
    t += dppz<0xb1>(t);
    t += dppz<0x4e>(t);
    return t;
}
DI u32x2 finish_tile(const f32x16& acc, float edge, u32x2 u, const ConvCtx& c) {
    f32x4 o;
    float cy;
    reduce_tile(acc, o, cy);
    cy = __int_as_float(__builtin_amdgcn_ds_bpermute(c.csrc, __float_as_int(cy)));
    const f32x4 uf = {bfhi(u[1]), bflo(u[1]), bfhi(u[0]), bflo(u[0])};
    o = uf * c.dv + o;
    o[0] += c.first ? edge : cy;
    return u32x2{pk(o[0], o[1]), pk(o[2], o[3])};
}
template <int PART>
DI void conv(char* smem, const ConvCtx& c, const u32x4 (&ea)[4], u32x4 a0) {
    constexpr int T1 = 7 - PART, T2 = PART == 0 ? 3 : 2 * PART, T3 = 2 - PART;
    constexpr int NS = PART == 2 ? 24 : 16, PF = PART == 2 ? 2 : 1;
    constexpr int F2 = 4 * T2 + 4 + PF, F3 = PART == 0 ? -9 : (PART == 1 ? 8 + PF : NS + PF);
    const char* pa = smem + c.pa;
    const char* pb = smem + c.pb;
    u32x4 fa[32], fb[32], fc[8];
    f32x16 acc1 = zero16(), acc2 = zero16(), acc3 = zero16();
    u32x4 eb[7];
    u32x2 u1, u2, u3, y2;
    f32x4 d1 = zero4(), d2 = zero4(), d3 = zero4();
    float e1, e2, e3 = 0.f;
    fa[0] = a0;
#define LDA(m) fa[m] = trread2(pa + 128 * (m), pa + 128 * (m) + 32)
#define BFR(n) (*(const u32x4*)(pb + 128 * ((n) + 3)))
#define LDB(n) fb[(n) + 3] = BFR(n)
#define EB(T, i) (*(const u32x4*)(smem + c.bz + 512 * (T) - 1024 * (i)))
#pragma unroll
    for (int i = 0; 2 * i < T1; ++i) eb[i] = EB(T1, i);
#pragma unroll
    for (int i = 0; 2 * i < T2; ++i) eb[4 + i] = EB(T2, i);
    if (PART == 1) eb[6] = EB(1, 0);
#pragma unroll
    for (int s = 0; s < NS + PF; ++s) {
        asm volatile("" ::: "memory");
        if (s < NS) {
            if (s >= 1) LDA(s);
            if (PART == 0) { LDB(28 - s); LDB(12 - s); LDA(s + 16); }
            if (PART == 1) { LDB(24 - s); if (s < 12) { LDB(8 - s); LDA(s + 16); } }
            if (PART == 2) { if (s < 4) LDB(20 - s); if (s < 20) LDB(16 - s); }
            if (PART == 1 && s < 8) fc[s] = BFR(4 - s);
            if (PART == 2 && s >= 20) fc[s - 20] = trread2(pa + 128 * (s - 20), pa + 128 * (s - 20) + 32);
        }
        if (s == F3 - 1) u3 = *(const u32x2*)(smem + c.pu + 512 * T3);
        if (s == F2 - 1) u2 = *(const u32x2*)(smem + c.pu + 512 * T2);
        if (s == NS + PF - 1) u1 = *(const u32x2*)(smem + c.pu + 512 * T1);
        __builtin_amdgcn_sched_barrier(0);
        if (s == 0) {
#pragma unroll
            for (int i = 0; 2 * i < T1; ++i) {
                d1 = mfma16(ea[i], eb[i], d1);
                if (2 * i < T2) d2 = mfma16(ea[i], eb[4 + i], d2);
            }
            if (PART == 1) d3 = mfma16(ea[0], eb[6], d3);
        }
        if (s >= PF) {
            const int m = s - PF;
            acc1 = mfma(fa[m], fb[4 * T1 - m + 3], acc1);
            if (m < 4 * T2 + 4) acc2 = mfma(fa[m], fb[4 * T2 - m + 3], acc2);
            if (PART < 2 && m < 4 * T2 + 4) acc1 = mfma(fa[m + 16], fb[4 * T2 - m + 3], acc1);
            if (PART == 1 && m < 8) acc3 = mfma(fa[m], fc[m], acc3);
            if (PART == 2 && m >= 20) acc3 = mfma(fc[m - 20], fb[20 - m + 3], acc3);
        }
        if (s == PF) {
            e1 = edge_sum(d1, c.diag);
            e2 = edge_sum(d2, c.diag);
            if (PART == 1) e3 = edge_sum(d3, c.diag);
        }
        if (s == F3) *(u32x2*)(smem + c.yo + (LB_U + 3584 - LB_Y)) = finish_tile(acc3, e3, u3, c);
        if (s == F2) y2 = finish_tile(acc2, e2, u2, c);
        __builtin_amdgcn_sched_barrier(0);
    }
#undef LDA
#undef LDB
#undef BFR
#undef EB
    if (PART == 0) y2 = finish_tile(acc2, e2, u2, c);
    if (PART == 2) *(u32x2*)(smem + c.yo + (LB_U + 3584 - LB_Y)) = finish_tile(acc3, e3, u3, c);
    const u32x2 y1 = finish_tile(acc1, e1, u1, c);
    *(u32x2*)(smem + c.yo + 512) = y2;
    *(u32x2*)(smem + c.yo) = y1;
}

constexpr int LA_EX = 10240, LA_E1 = 28672, LA_T3 = 53248, LA_T1 = 67584;

__global__ __launch_bounds__(512, 4) void k_fused(const u16* __restrict__ x, const u16* __restrict__ wr, const u16* __restrict__ wi,
                                                  const u16* __restrict__ qq, const u16* __restrict__ kk, const u16* __restrict__ vv,
                                                  const u16* __restrict__ shk, const u16* __restrict__ hk, const u16* __restrict__ dd,
                                                  u16* __restrict__ out, char* ws) {
    __shared__ __attribute__((aligned(16))) char smem[LDS_BYTES];
    const int tid = threadIdx.x, lane = tid & 63;
    const int w = __builtin_amdgcn_readfirstlane(tid >> 6);
    const int n = lane & 31, hh = lane >> 5;
    const int g = lane >> 4, q = (lane >> 2) & 3, p = lane & 3;
    const unsigned lb = (unsigned)(size_t)(lds_vp)smem;
    STAMP_DECL
    STAMP(0);
    unsigned warm = 0;
    if (w >= 5 && blockIdx.x < CODE_WGS) {
        unsigned long long pc;
        asm volatile("s_getpc_b64 %0" : "=s"(pc));
        asm volatile("global_load_dword %0, %1, %2" : "=v"(warm) : "v"(min((tid - 320) * 128, CODE_BYTES)), "s"(pc) : "memory");
    }
    asm volatile("" :: "s"(x), "s"(wr), "s"(wi), "s"(qq), "s"(kk), "s"(vv), "s"(shk), "s"(hk), "s"(dd), "s"(out), "s"(ws));
    const rsrc_t rs = mkrs(ws, WS_END);
#define rs0 rs
    if (blockIdx.x < 256) {
        const int i2 = blockIdx.x;
        const bool hasU = i2 < 8 * 27;
        if (w < 4) {
            {
                const char* r0 = (const char*)(x + (size_t)(256 * (2 * w) + i2) * D_);
                dma16(r0 + lane * 16, lb + w * 2560);
                dma16(r0 + 256 * D_ * 2 + lane * 16, lb + w * 2560 + 1024);
                if (lane < 32) dma16(r0 + g * (256 * D_ * 2) + 1024 + (lane & 15) * 16, lb + w * 2560 + 2048);
            }
            unsigned ep = ld4(rs0, WS_EP, 0);
            u32x4 c1m[2];
#pragma unroll
            for (int t = 0; t < 4; ++t) {
                unsigned f = cs((float)((256 * (4 * hh + t) + i2) * (n >> 2)) * (1.0f / 2048.0f) + 0.25f * (n & 1));
                c1m[0][t] = (n & 2) ? 0u : f;
                c1m[1][t] = (n & 2) ? f : 0u;
            }
            const int pi = min(28 * w + n, PK - 1);
            int exr[3];
#pragma unroll
            for (int e = 0; e < 3; ++e) {
                int kd = min(3 * pi + e, 320);
                int m = (kd * 1639) >> 16, c = kd - 40 * m;
                int sa = 21 * m + c, sb = 355 - sa;
                exr[e] = LA_EX + (c <= 20 ? sa : sb) * 48 + 16 * hh;
                PIN(exr[e]);
            }
            PIN(c1m[0]); PIN(c1m[1]);
            int xao = w * 2560 + (g & 1) * 1024 + (8 * hh + q) * 32 + p * 8;
            int xto = w * 2560 + 2048 + (g & 1) * 256 + q * 32 + p * 8;
            int exw = LA_EX + (42 * hh + n) * 48 + 8 * w;
            int l16 = lane * 16 + 32768;
            unsigned rowoff = i2 * ROWB + pi * 16;
            unsigned x1o = (n < 28 && 28 * w + n < PK) ? WS_X1 + (i2 >> 1) * (2 * ROWB) + (i2 & 1) * 64 + (pi >> 2) * 128 + (pi & 3) * 16 + hh * (PK * 32) : 0x80000000u;
            PIN(xao); PIN(xto); PIN(exw); PIN(x1o); PIN(l16);
            STAMP(1);
            __builtin_amdgcn_s_setprio(2);
            PIN(ep);
            const unsigned tag = 2u * __builtin_amdgcn_readfirstlane(ep) + 3u;
            DRAIN();
            BAR();
            STAMP(2);
            {
                const char* xa = smem + xao;
                const char* xt = smem + xto;
                const char* tp = smem + l16 + (LA_T1 - 32768);
                u32x4 tb[12];
                u32x4 a0 = trread2(xa, xa + 128);
                tb[0] = *(const u32x4*)tp; tb[1] = *(const u32x4*)(tp + 1024);
                u32x4 a1 = trread2(xa + 512, xa + 640);
                tb[2] = *(const u32x4*)(tp + 2048); tb[3] = *(const u32x4*)(tp + 3072);
                u32x4 a2 = trread2(xt, xt + 128);
                tb[4] = *(const u32x4*)(tp + 4096); tb[5] = *(const u32x4*)(tp + 5120);
                __builtin_amdgcn_sched_barrier(0);
                f32x16 tr = mfma(a0, tb[0], zero16());
                f32x16 ti = mfma(a0, tb[1], zero16());
                __builtin_amdgcn_sched_barrier(0);
#pragma unroll
                for (int f = 11; f >= 6; --f) tb[f] = *(const u32x4*)(tp + f * 1024);
                __builtin_amdgcn_sched_barrier(0);
                tr = mfma(a1, tb[2], tr);
                ti = mfma(a1, tb[3], ti);
                tr = mfma(a2, tb[4], tr);
                ti = mfma(a2, tb[5], ti);
                f32x16 y[2];
#pragma unroll
                for (int ss = 0; ss < 2; ++ss) {
                    f32x4 r0 = sub4(tr, 8 * ss), r1 = sub4(tr, 8 * ss + 4), i0 = sub4(ti, 8 * ss), i1 = sub4(ti, 8 * ss + 4);
                    f32x4 m2 = r0 * asf(tb[10]), m3 = r1 * asf(tb[11]), m0 = i0 * asf(tb[10]), m1 = i1 * asf(tb[11]);
                    __builtin_amdgcn_sched_barrier(0);
                    m0 = r0 * asf(tb[8]) + m0; m1 = r1 * asf(tb[9]) + m1;
                    m2 = i0 * asf(tb[8]) - m2; m3 = i1 * asf(tb[9]) - m3;
                    __builtin_amdgcn_sched_barrier(0);
                    u32x4 pr = pk44(m0, m1), pi_ = pk44(m2, m3);
                    __builtin_amdgcn_sched_barrier(0);
                    y[ss] = mfma(tb[6], pr, zero16());
                    y[ss] = mfma(tb[7], pi_, y[ss]);
                }
                if (n <= 20) {
#pragma unroll
                    for (int t = 0; t < 8; ++t) {
                        u32x2 v = {pk(y[0][2 * t], y[0][2 * t + 1]), pk(y[1][2 * t], y[1][2 * t + 1])};
                        *(u32x2*)(smem + exw + 21 * ((t & 1) + 4 * (t >> 1)) * 48) = v;
                    }
                }
            }
            BAR();
            {
                const u32x4 b0 = *(const u32x4*)(smem + exr[0]), b2 = *(const u32x4*)(smem + exr[2]), b1 = *(const u32x4*)(smem + exr[1]);
                __builtin_amdgcn_sched_barrier(0);
                f32x16 o0 = mfma(c1m[0], b0, zero16());
                f32x16 o1 = mfma(c1m[0], b2, zero16());
                o0 = mfma(c1m[1], b1, o0);
                u32x4 pc[4];
#pragma unroll
                for (int gq = 0; gq < 4; ++gq)
                    pc[gq] = piece(pk(o0[4 * gq], o0[4 * gq + 1]), pk(o0[4 * gq + 2], o0[4 * gq + 3]), pk(o1[4 * gq], o1[4 * gq + 1]), tag);
#pragma unroll
                for (int gq = 0; gq < 4; ++gq) st16(rs, x1o, gq * 2 * PK * 32, pc[gq]);
            }
            STAMP(3);
            DRAIN();
            STAMP(4);
            BAR();
            u32x4 tc[14];
#pragma unroll
            for (int f = 0; f < 14; ++f) tc[f] = *(const u32x4*)(smem + l16 + (LA_T3 - 32768) + f * 1024);
            u32x4 ci[2];
#pragma unroll
            for (int t = 0; t < 4; ++t) {
                unsigned f = cs(0.25f * (n & 1) - (float)((256 * (n >> 2) + i2) * (4 * hh + t)) * (1.0f / 2048.0f));
                ci[0][t] = (n & 2) ? 0u : f;
                ci[1][t] = (n & 2) ? f : 0u;
            }
            PIN(ci[0]); PIN(ci[1]);
#pragma unroll
            for (int f = 0; f < 14; ++f) PIN(tc[f]);
            unsigned zoff = WS_Z1 + rowoff + 4 * hh * (PK * 16);
            const int k1c = min(n, 20);
            int e3o = (hh * 324 + 3 * pi) * 4, s0o = (2 * w * 324 + 160 * hh + k1c) * 4, s1o = (2 * w * 324 + 200 - 160 * hh - k1c) * 4;
            unsigned oo = ((256 * 2 * w + i2) * (2 * D_) + 16 * n + 8 * hh) * 2;
            unsigned oo2 = n < 8 ? oo + 1024 : 0x80000000u;
            PIN(zoff); PIN(e3o); PIN(s0o); PIN(s1o); PIN(oo); PIN(oo2);
            const rsrc_t ro = mkrs(out, S_ * 2 * D_ * 2);
            if (hasU) {
                BAR(); BAR(); BAR();
            } else {
#pragma unroll 1
                for (unsigned spins = 0; spins < SPIN_CAP; ++spins) {
                    __builtin_amdgcn_s_sleep(4);
                    asm volatile("" ::: "memory");
                    u32x4 v = ld16(rs, WS_Z1 + i2 * ROWB + w * 2 * PK * 16, 0);
                    if (__builtin_amdgcn_readfirstlane(v[0] ^ v[1] ^ v[2] ^ v[3]) == tag) break;
                }
            }
            STAMP(9);
            {
                u32x4 zb[3];
                fetch4(rs, zoff, PK * 16, tag, zb);
                STAMP(10);
                f32x16 o0 = mfma(ci[0], zb[0], zero16());
                f32x16 o1 = mfma(ci[0], zb[2], zero16());
                o0 = mfma(ci[1], zb[1], o0);
                unsigned* e3 = (unsigned*)(smem + e3o);
#pragma unroll
                for (int gq = 0; gq < 4; ++gq) {
                    e3[gq * 648] = pk(o0[4 * gq], o0[4 * gq + 1]);
                    e3[gq * 648 + 1] = pk(o0[4 * gq + 2], o0[4 * gq + 3]);
                    e3[gq * 648 + 2] = pk(o1[4 * gq], o1[4 * gq + 1]);
                }
            }
            BAR();
            if (i2 == 0 && tid == 0) st4(rs0, WS_EP, 0, ep + 1u);
            {
                const unsigned* s0 = (const unsigned*)(smem + s0o);
                const unsigned* s1 = (const unsigned*)(smem + s1o);
                const u32x4 f0 = {s0[0], s0[40], s0[80], s0[120]}, f1 = {s0[324], s0[364], s0[404], s0[444]};
                const u32x4 f2 = {s1[120], s1[80], s1[40], s1[0]}, f3 = {s1[444], s1[404], s1[364], s1[324]};
                __builtin_amdgcn_sched_barrier(0);
                f32x16 tr = mfma(f0, tc[0], zero16());
                f32x16 ti = mfma(f1, tc[0], zero16());
                tr = mfma(f2, tc[1], tr);
                ti = mfma(f3, tc[1], ti);
                f32x4 re[3], im[3], mr[3], mi[3];
#pragma unroll
                for (int r = 0; r < 12; ++r) {
                    auto sw = __builtin_amdgcn_permlane16_swap(__float_as_uint(tr[r]), __float_as_uint(ti[r]), false, false);
                    re[r >> 2][r & 3] = __uint_as_float(sw[0]);
                    im[r >> 2][r & 3] = __uint_as_float(sw[1]);
                }
                __builtin_amdgcn_sched_barrier(0);
#pragma unroll
                for (int j = 0; j < 3; ++j) { mr[j] = im[j] * asf(tc[11 + j]); mi[j] = re[j] * asf(tc[11 + j]); }
                __builtin_amdgcn_sched_barrier(0);
#pragma unroll
                for (int j = 0; j < 3; ++j) { mr[j] = re[j] * asf(tc[8 + j]) - mr[j]; mi[j] = im[j] * asf(tc[8 + j]) + mi[j]; }
                __builtin_amdgcn_sched_barrier(0);
                const u32x4 a0 = pk44(mr[0], mr[1]), a1 = pk44(mi[0], mi[1]), a2 = pk44(mr[2], mi[2]);
                f32x16 y0 = mfma(a0, tc[2], zero16());
                y0 = mfma(a1, tc[3], y0);
                y0 = mfma(a2, tc[4], y0);
                f32x16 y1 = mfma(a0, tc[5], zero16());
                y1 = mfma(a1, tc[6], y1);
                y1 = mfma(a2, tc[7], y1);
                __builtin_amdgcn_raw_buffer_store_b128(pack8(y0, 0), ro, oo, 0, OUT_AUX);
                __builtin_amdgcn_raw_buffer_store_b128(pack8(y0, 8), ro, oo, 256 * 2 * D_ * 2, OUT_AUX);
                __builtin_amdgcn_raw_buffer_store_b128(pack8(y1, 0), ro, oo2, 0, OUT_AUX);
                __builtin_amdgcn_raw_buffer_store_b128(pack8(y1, 8), ro, oo2, 256 * 2 * D_ * 2, OUT_AUX);
            }
            STAMP(11);
        } else {
            const int w2 = w - 4;
#pragma unroll
            for (int k = 0; k < 3; ++k) dma16((const char*)g_t1.v + (3 * w2 + k) * 1024 + lane * 16, lb + LA_T1 + (3 * w2 + k) * 1024);
            const unsigned ep = ld4(rs0, WS_EP, 0);
            const int k1s = i2 & 7, pi = 4 * (i2 >> 3) + p;
            const unsigned we = (4 * w2 + q + 64 * g) * NW + k1s * DF + 3 * pi;
            unsigned vo = hasU ? (we * 2) & ~3u : 0x80000000u;
            PIN(vo);
            u32x4 fre, fim, fin;
            f32x4 tc, ts;
#pragma unroll
            for (int t = 0; t < 4; ++t) {
                float an = (float)((lane & 15) * (4 * g + t)) * (1.0f / 16.0f);
                float c = fcos(an), s = fsin(an);
                fre[t] = pk(c, s);
                fim[t] = pk(-s, c);
                fin[t] = fim[t] ^ 0x80008000u;
                float b = (float)((4 * w2 + q) * (4 * g + t)) * (1.0f / 256.0f);
                tc[t] = fcos(b); ts[t] = fsin(b);
            }
            PIN(fre); PIN(fim); PIN(fin); PIN(tc); PIN(ts);
            DRAIN();
            BAR();
            asm volatile("" :: "v"(warm));
            u32x2 wraw[4][2];
            {
                const rsrc_t rwr = mkrs(wr, S_ * DF * 2), rwi = mkrs(wi, S_ * DF * 2);
#pragma unroll
                for (int r = 0; r < 4; ++r) {
                    wraw[r][0] = __builtin_amdgcn_raw_buffer_load_b64(rwr, vo, r * 16 * NW * 2, 0);
                    wraw[r][1] = __builtin_amdgcn_raw_buffer_load_b64(rwi, vo, r * 16 * NW * 2, 0);
                }
            }
#pragma unroll
            for (int k = 0; k < 4; ++k) {
                const int f = min(4 * w2 + k, 13);
                dma16((const char*)g_t3.v + f * 1024 + lane * 16, lb + LA_T3 + f * 1024);
            }
            BAR();
            DRAIN();
            f32x4 wf[2][3];
            {
                const unsigned sh = (we & 1) * 16;
#pragma unroll
                for (int r = 0; r < 4; ++r)
#pragma unroll
                    for (int a = 0; a < 2; ++a) {
                        unsigned lo = __builtin_amdgcn_alignbit(wraw[r][a][1], wraw[r][a][0], sh), hi = wraw[r][a][1] >> sh;
                        wf[a][0][r] = bflo(lo); wf[a][1][r] = bfhi(lo); wf[a][2][r] = bflo(hi);
                    }
#pragma unroll
                for (int e = 0; e < 3; ++e) { PIN(wf[0][e]); PIN(wf[1][e]); }
            }
            const unsigned tag = 2u * __builtin_amdgcn_readfirstlane(ep) + 3u;
            unsigned coloff = (64 * g + 4 * w2 + q) * ROWB + (k1s * PK + pi) * 16;
            int ewo = LA_E1 + (g * 768 + (16 * w2 + p) * 4 + q) * 4, ero = LA_E1 + w2 * 3072 + lane * 16;
            unsigned xoff = WS_X1 + (64 * g + 4 * w2 + (q & 2)) * ROWB + (k1s * 27 + (i2 >> 3)) * 128 + (lane & 7) * 16;
            PIN(coloff); PIN(ewo); PIN(ero); PIN(xoff);
            unsigned* ew = (unsigned*)(smem + ewo);
            const char* er = smem + ero;
            STAMP(1);
            __builtin_amdgcn_s_setprio(2);
            BAR();
            if (hasU) {
            STAMP(5);
            u32x4 f[3];
            f32x4 re[3], im[3], mr[3], mi[3];
            fetch4(rs, xoff, 16 * ROWB, tag, f);
            STAMP(6);
#define DFT16(FI) _Pragma("unroll") for (int e = 0; e < 3; ++e) { re[e] = mfma16(fre, f[e], zero4()); im[e] = mfma16(FI, f[e], zero4()); }
#define TWID_STORE(OFF) \
            _Pragma("unroll") for (int e = 0; e < 3; ++e) { \
                mr[e] = im[e] * ts; mi[e] = re[e] * ts; \
                mr[e] = re[e] * tc + mr[e]; mi[e] = im[e] * tc - mi[e]; \
                u32x4 v = pkc(mr[e], mi[e]); \
                _Pragma("unroll") for (int r = 0; r < 4; ++r) ew[OFF + e * 256 + r * 16] = v[r]; \
            }
            DFT16(fim);
            __builtin_amdgcn_sched_barrier(0);
            TWID_STORE(0);
            BAR();
#pragma unroll
            for (int e = 0; e < 3; ++e) f[e] = *(const u32x4*)(er + e * 1024);
            __builtin_amdgcn_sched_barrier(0);
            DFT16(fin);
            __builtin_amdgcn_sched_barrier(0);
#pragma unroll
            for (int e = 0; e < 3; ++e) {
                mr[e] = im[e] * wf[1][e]; mi[e] = re[e] * wf[1][e];
                mr[e] = re[e] * wf[0][e] + mr[e]; mi[e] = im[e] * wf[0][e] - mi[e];
                f[e] = pkc(mr[e], mi[e]);
                re[e] = mfma16(fre, f[e], zero4()); im[e] = mfma16(fim, f[e], zero4());
            }
            __builtin_amdgcn_sched_barrier(0);
            TWID_STORE(3072);
            BAR();
#pragma unroll
            for (int e = 0; e < 3; ++e) f[e] = *(const u32x4*)(er + 12288 + e * 1024);
            __builtin_amdgcn_sched_barrier(0);
            DFT16(fin);
            __builtin_amdgcn_sched_barrier(0);
#pragma unroll
            for (int e = 0; e < 3; ++e) f[e] = pkc(re[e], im[e]);
#pragma unroll
            for (int r = 0; r < 4; ++r) st16(rs, coloff, WS_Z1 + r * 16 * ROWB, piece(f[0][r], f[1][r], f[2][r], tag));
            STAMP(7);
            DRAIN();
            STAMP(8);
            BAR();
            }
        }
    } else {
        const int bb = blockIdx.x - 256;
        const int xg = bb & 7, ib = bb >> 3;
        const bool cons = ib < 30;
        const int gl = min(ib / 3, 9), part = ib - 3 * (ib / 3);
        const int ch0 = 80 * xg + 8 * gl;
        const unsigned epo = WS_EP + 256 * (1 + xg);
        unsigned ep;
        if (w < 5) {
            __builtin_amdgcn_s_setprio(1);
            const int rg = tid / 40, cp = tid - 40 * rg;
            const int chp = 80 * xg + 2 * cp, s0 = 64 * ib + 8 * rg;
            const rsrc_t rk = mkrs(kk, S_ * D_ * 2), rv = mkrs(vv, S_ * D_ * 2);
            const unsigned vo = (s0 * D_ + chp) * 2;
            const unsigned ho = s0 > 0 ? vo - 3 * D_ * 2 : 0x80000000u;
            unsigned kr[11], vr[8];
            const u32x4 sw = *(const u32x4*)(shk + chp * 4);
#pragma unroll
            for (int i = 0; i < 3; ++i) kr[i] = __builtin_amdgcn_raw_buffer_load_b32(rk, ho + i * D_ * 2, 0, 0);
#pragma unroll
            for (int i = 0; i < 8; ++i) {
                kr[3 + i] = __builtin_amdgcn_raw_buffer_load_b32(rk, vo + (i % 3) * D_ * 2, (i / 3) * 3 * D_ * 2, 0);
                vr[i] = __builtin_amdgcn_raw_buffer_load_b32(rv, vo + (i % 3) * D_ * 2, (i / 3) * 3 * D_ * 2, 0);
            }
            ep = ld4(rs, epo, 0);
            __builtin_amdgcn_sched_barrier(0);
            STAMP(1);
            f32x2 wt[4] = {{bflo(sw[0]), bflo(sw[2])}, {bfhi(sw[0]), bfhi(sw[2])}, {bflo(sw[1]), bflo(sw[3])}, {bfhi(sw[1]), bfhi(sw[3])}};
            f32x2 kf[11], o[8];
#pragma unroll
            for (int i = 0; i < 11; ++i) {
                kf[i] = f32x2{bflo(kr[i]), bfhi(kr[i])};
            }
#pragma unroll
            for (int e = 0; e < 8; ++e) {
                f32x2 a = kf[e] * wt[3];
                a = kf[e + 1] * wt[2] + a;
                a = kf[e + 2] * wt[1] + a;
                a = kf[e + 3] * wt[0] + a;
                o[e] = a * f32x2{bflo(vr[e]), bfhi(vr[e])};
            }
#pragma unroll
            for (int c = 0; c < 2; ++c) {
                char* dst = smem + LB_ST + (c * 40 + cp) * 136 + rg * 16;
                *(u32x2*)dst = u32x2{pk(o[0][c], o[1][c]), pk(o[2][c], o[3][c])};
                *(u32x2*)(dst + 8) = u32x2{pk(o[4][c], o[5][c]), pk(o[6][c], o[7][c])};
            }
            PIN(ep);
            STAMP(2);
            BAR();
            const char* src = smem + LB_ST + ((p >> 1) * 40 + 8 * w + g) * 136 + 64 * (p & 1) + 8 * (7 - q);
            const unsigned mo = WS_KV + ((xg * 10 + 2 * w) * 32 + ib) * MSGB;
            const u32x4 u0 = trread2(src, src - 32), u1 = trread2(src + 4 * 136, src + 4 * 136 - 32);
            st16(rs, lane * 16, mo, u0);
            st16(rs, lane * 16, mo + 32 * MSGB, u1);
            STAMP(3);
            DRAIN();
            if (lane < 2) st4(rs, lane * (32 * MSGB) + 1024, mo, 2u * ep + 3u);
            __builtin_amdgcn_s_setprio(0);
            STAMP(4);
        } else {
            ep = ld4(rs, epo, 0);
            const int t3 = tid - 320;
#pragma unroll
            for (int k = 0; k < 2; ++k) {
                const int z = t3 + 192 * k, c = z / 36, o = z - 36 * c;
                if (z < 288) *(u32x4*)(smem + c * CHB + (o < 4 ? o * 16 : LB_Z + (o - 4) * 16)) = u32x4{0u, 0u, 0u, 0u};
            }
            PIN(ep);
            if (cons) {
                __builtin_amdgcn_s_sleep(DLY_H);
                const char* hp = (const char*)(hk + (size_t)ch0 * S_) + lane * 16;
#pragma unroll 1
                for (int k = w - 5; k < 32; k += 3) dma16(hp + k * 1024, lb + (k >> 2) * CHB + LB_H + (k & 3) * 1024);
            }
            BAR();
            DRAIN();
        }
        if (cons) {
            const unsigned tag = 2u * __builtin_amdgcn_readfirstlane(ep) + 3u;
            ConvCtx c;
            {
                const int pi = 2 * (g & 1) + (p >> 1) + 4 * (p & 1);
                const int a = (n & 3) | ((n >> 4) << 2), am = (a + 7) & 7;
                c.pa = w * CHB + 8 + 8 * (8 * hh + q + pi);
                c.pb = w * CHB + LB_U + 64 * (a - hh) + 16 * q - 384;
                c.bz = w * CHB + LB_U + 16 * q - 64 * (1 + 4 * p + g);
                c.pu = w * CHB + LB_U + 64 * a + 16 * q + 8 - 8 * hh;
                c.csrc = 4 * ((hh ? n : ((am & 3) | (q << 2) | ((am >> 2) << 4))) + 32 - 32 * hh);
                c.yo = w * CHB + LB_Y + lane * 8;
                c.dv = bflo(dd[ch0 + w]);
                c.first = (lane & 0x33) == 0;
                c.diag = g == p;
                PIN(c.pa); PIN(c.pb); PIN(c.bz); PIN(c.pu); PIN(c.csrc); PIN(c.yo); PIN(c.dv);
            }
            const int nk = (32 - 4 * part - w + 7) >> 3;
            const unsigned mo = WS_KV + ((xg * 10 + gl) * 32 + w) * MSGB;
            const unsigned m3 = (nk - 1) * 8;
            unsigned rowa, rowb;
            {
                const int T1 = 7 - part, T2 = part == 0 ? 3 : 2 * part, T3 = 2 - part;
                const int lq = (tid >> 2) & 63, nq = lq & 31;
                const int row3 = 32 * ((nq & 3) | ((nq >> 4) << 2)) + 16 * (lq >> 5) + ((nq >> 2) & 3) + 4 * p;
                rowa = (row3 + 256 * (w < 4 ? T1 : T2)) * (D_ * 2);
                rowb = (row3 + ((part > 0 && w < 4) ? 256 * T3 : 0x100000)) * (D_ * 2);
            }
            int uo = (lane >> 3) * CHB + LB_U + w * 128 + (lane & 7) * 16;
            int yso = q * CHB + LB_Y + (w * 64 + g * 16 + p * 4) * 2;
            unsigned fo = min(p, nk - 1) * 8 * MSGB + 1024;
            int bao = w * CHB + LB_H + 8 * (32 * p + 8 * g + q);
            unsigned qoa = rowa, qob = rowb;
            PIN(rowa); PIN(rowb); PIN(uo); PIN(yso); PIN(fo); PIN(bao);
            BAR();
            auto tail = [&](auto part_c) __attribute__((always_inline)) {
            constexpr int PART = decltype(part_c)::v;
            u32x4 ea[4];
#pragma unroll
            for (int i = 0; i < 4; ++i) { ea[i] = trread2(smem + bao + 1024 * i, smem + bao + 1024 * i + 32); PIN(ea[i]); }
            u32x4 a0 = trread2(smem + c.pa, smem + c.pa + 32);
            PIN(a0);
#pragma unroll 1
            for (unsigned spins = 0; spins < SPIN_CAP; ++spins) {
                asm volatile("" ::: "memory");
                unsigned fl = ld4(rs, fo, mo);
                COUNT(13);
                if (__builtin_amdgcn_ballot_w64(fl != tag) == 0) break;
                __builtin_amdgcn_s_sleep(2);
            }
            asm volatile("" ::: "memory");
            STAMP(5);
            {
                u32x4 um[4];
#pragma unroll
                for (int k = 0; k < 4; ++k) um[k] = ld16b(rs, lane * 16, mo + (k < 3 ? 8 * k : m3) * MSGB);
#pragma unroll
                for (int k = 0; k < 3; ++k) *(u32x4*)(smem + uo + 1024 * k) = um[k];
                *(u32x4*)(smem + uo + 128 * m3) = um[3];
            }
            STAMP(6);
            BAR();
            STAMP(9);
            const rsrc_t rq = mkrs(qq, S_ * D_ * 2), ro = mkrs(out, S_ * 2 * D_ * 2);
            const u32x4 qa = __builtin_amdgcn_raw_buffer_load_b128(rq, qoa, ch0 * 2, 0);
            const u32x4 qb = __builtin_amdgcn_raw_buffer_load_b128(rq, qob, ch0 * 2, 0);
            __builtin_amdgcn_sched_barrier(0);
            conv<PART>(smem, c, ea, a0);
            STAMP(7);
            BAR();
            {
                const char* ys = smem + yso;
                const u32x4 y = trread2(ys, ys + 4 * CHB), z = trread2(ys + (LB_U + 3584 - LB_Y), ys + (LB_U + 3584 - LB_Y) + 4 * CHB);
                u32x4 o;
#pragma unroll
                for (int k = 0; k < 4; ++k) o[k] = pk(bflo(y[k]) * bflo(qa[k]), bfhi(y[k]) * bfhi(qa[k]));
                __builtin_amdgcn_raw_buffer_store_b128(o, ro, 2 * rowa, (D_ + ch0) * 2, 0);
#pragma unroll
                for (int k = 0; k < 4; ++k) o[k] = pk(bflo(z[k]) * bflo(qb[k]), bfhi(z[k]) * bfhi(qb[k]));
                __builtin_amdgcn_raw_buffer_store_b128(o, ro, 2 * rowb, (D_ + ch0) * 2, 0);
            }
            };
            if (part == 0) tail(IC<0>{});
            else if (part == 1) tail(IC<1>{});
            else tail(IC<2>{});
            if (ib == 0 && tid == 0) st4(rs, epo, 0, ep + 1u);
            STAMP(8);
        }
    }
    STAMP_OUT;
}
extern "C" void kernel_launch(void* const* d_in, const int* in_sizes, int n_in, void* d_out, int out_size, void* d_ws,
                              size_t ws_size, hipStream_t stream) {
    k_fused<<<512, 512, 0, stream>>>((const u16*)d_in[0], (const u16*)d_in[1], (const u16*)d_in[2], (const u16*)d_in[3],
                                     (const u16*)d_in[4], (const u16*)d_in[5], (const u16*)d_in[6], (const u16*)d_in[7],
                                     (const u16*)d_in[8], (u16*)d_out, (char*)d_ws);
}
```

```cpp
#define WSOFF ((ws_size - (16u << 20)) & ~(size_t)0x1fffff)
#define NT_X 0
#define NT_KV 0
#define NT_W 0
#define NT_H 0
#define NT_Q 0
#include <hip/hip_runtime.h>
#include <hip/hip_bf16.h>

typedef __attribute__((ext_vector_type(4))) short s16x4;
typedef __attribute__((ext_vector_type(2))) __bf16 bf16x2;
typedef __attribute__((ext_vector_type(8))) __bf16 bf16x8;
typedef __attribute__((ext_vector_type(2))) float f32x2;
typedef __attribute__((ext_vector_type(4))) float f32x4;
typedef __attribute__((ext_vector_type(16))) float f32x16;
typedef __attribute__((ext_vector_type(2))) unsigned u32x2;
typedef __attribute__((ext_vector_type(4))) unsigned u32x4;
typedef __attribute__((address_space(3))) s16x4* lds_s16x4p;
typedef __attribute__((address_space(3))) void* lds_vp;
typedef unsigned short u16;
typedef __amdgpu_buffer_rsrc_t rsrc_t;

#define DI __device__ __forceinline__

#define STAMP(I)
#define STAMP_DECL
#define STAMP_OUT
#define COUNT(I)

namespace tab {
constexpr double PI = 3.14159265358979323846;
constexpr double csin(double x) { double t = x, s = x; for (int i = 1; i < 12; ++i) { t *= -x * x / ((2 * i) * (2 * i + 1)); s += t; } return s; }
constexpr double ccos(double x) { double t = 1, s = 1; for (int i = 1; i < 12; ++i) { t *= -x * x / ((2 * i - 1) * (2 * i)); s += t; } return s; }
struct Base { float c[640]; };
constexpr Base mkbase() {
    Base b{};
    for (int i = 0; i < 640; ++i) {
        int m = i > 320 ? 640 - i : i;
        b.c[i] = m <= 160 ? (m <= 80 ? (float)ccos(2 * PI * m / 640) : (float)csin(2 * PI * (160 - m) / 640))
                          : (m >= 240 ? (float)-ccos(2 * PI * (320 - m) / 640) : (float)-csin(2 * PI * (m - 160) / 640));
    }
    return b;
}
constexpr Base base = mkbase();
constexpr float C(int n) { return base.c[((n % 640) + 640) % 640]; }
constexpr float S(int n) { return C(n - 160); }
constexpr unsigned fb(float f) { return __builtin_bit_cast(unsigned, f); }
constexpr unsigned bf(float f) { unsigned u = fb(f); return (u + 0x7fffu + ((u >> 16) & 1u)) >> 16; }
constexpr unsigned p2(float lo, float hi) { return bf(lo) | (bf(hi) << 16); }
struct T1 { unsigned v[12 * 256]; };
struct T3 { unsigned v[14 * 256]; };
constexpr T1 mk1() {
    T1 t{};
    for (int l = 0; l < 64; ++l) {
        const int n = l & 31, hh = l >> 5;
        for (int k = 0; k < 4; ++k) {
            for (int sg = 0; sg < 3; ++sg) {
                const int d1 = 16 * sg + 8 * hh + 2 * k;
                const bool ok = d1 < 40;
                t.v[((2 * sg) * 64 + l) * 4 + k] = ok ? p2(C(16 * d1 * n), C(16 * (d1 + 1) * n)) : 0u;
                t.v[((2 * sg + 1) * 64 + l) * 4 + k] = ok ? p2(-S(16 * d1 * n), -S(16 * (d1 + 1) * n)) : 0u;
            }
            const int k2 = n >> 1, ph = (n & 1) ? (k2 >= 8 ? -160 : 160) : 0;
            const int d2 = 2 * (k & 1) + 8 * (k >> 1) + 4 * hh;
            t.v[(6 * 64 + l) * 4 + k] = p2(C(40 * d2 * k2 + ph), C(40 * (d2 + 1) * k2 + ph));
            t.v[(7 * 64 + l) * 4 + k] = p2(S(40 * d2 * k2 + ph), S(40 * (d2 + 1) * k2 + ph));
            for (int e = 0; e < 2; ++e) {
                t.v[((8 + e) * 64 + l) * 4 + k] = fb(C((k + 8 * e + 4 * hh) * n));
                t.v[((10 + e) * 64 + l) * 4 + k] = fb(S((k + 8 * e + 4 * hh) * n));
            }
        }
    }
    return t;
}
constexpr float wt(int k1) { return k1 > 20 ? 0.f : ((k1 == 0 || k1 == 20) ? 1.0f : 2.0f) * (1.0f / (2048.0f * 640.0f)); }
constexpr T3 mk3() {
    T3 t{};
    for (int l = 0; l < 64; ++l) {
        const int n = l & 31, hh = l >> 5, e = n & 15, ro = n >> 4;
        const int d2 = (e & 3) + 8 * ((e >> 2) & 1) + 4 * (e >> 3);
        for (int k = 0; k < 4; ++k) {
            const int a0 = 160 * ro - 40 * d2 * (4 * hh + k), a1 = 40 * d2 * (8 + 4 * hh + k) - 160 * ro;
            t.v[(0 * 64 + l) * 4 + k] = p2(C(a0), S(a0));
            t.v[(1 * 64 + l) * 4 + k] = p2(C(a1), S(a1));
            for (int tl = 0; tl < 2; ++tl) {
                const int d1 = 32 * tl + n;
                const int ka = 2 * (k & 1) + 8 * (k >> 1) + 4 * hh, kb = 16 + 2 * (k & 1) + 4 * hh;
                t.v[((2 + 3 * tl) * 64 + l) * 4 + k] = p2(wt(ka) * C(16 * d1 * ka), wt(ka + 1) * C(16 * d1 * (ka + 1)));
                t.v[((3 + 3 * tl) * 64 + l) * 4 + k] = p2(-wt(ka) * S(16 * d1 * ka), -wt(ka + 1) * S(16 * d1 * (ka + 1)));
                t.v[((4 + 3 * tl) * 64 + l) * 4 + k] = k < 2 ? p2(wt(kb) * C(16 * d1 * kb), wt(kb + 1) * C(16 * d1 * (kb + 1)))
                                                             : p2(-wt(kb) * S(16 * d1 * kb), -wt(kb + 1) * S(16 * d1 * (kb + 1)));
            }
            for (int j = 0; j < 3; ++j) {
                t.v[((8 + j) * 64 + l) * 4 + k] = fb(C(d2 * (k + 8 * j + 4 * hh)));
                t.v[((11 + j) * 64 + l) * 4 + k] = fb(S(d2 * (k + 8 * j + 4 * hh)));
            }
        }
    }
    return t;
}
}
__device__ __attribute__((aligned(128))) const tab::T1 g_t1 = tab::mk1();
__device__ __attribute__((aligned(128))) const tab::T3 g_t3 = tab::mk3();

constexpr int S_ = 2048, D_ = 640, DF = 321;
constexpr int NW = 8 * DF;
constexpr int PK = 108;
constexpr int ROWB = 8 * PK * 16;
constexpr int MSGB = 1088;
constexpr unsigned WS_X1 = 0, WS_Z1 = 4u << 20, WS_KV = 8u << 20, WS_EP = 12u << 20, WS_END = 13u << 20;
constexpr unsigned SPIN_CAP = 1u << 17;
constexpr int CHB = 8832, LB_H = 64, LB_Y = 2112, LB_Z = 4160, LB_U = 4672, LB_ST = 8 * CHB, LDS_BYTES = LB_ST + 80 * 136;

DI unsigned pk(float lo, float hi) {
    f32x2 v = {lo, hi};
    return __builtin_bit_cast(unsigned, __builtin_convertvector(v, bf16x2));
}
DI float bflo(unsigned u) { return __uint_as_float(u << 16); }
DI float bfhi(unsigned u) { return __uint_as_float(u & 0xffff0000u); }
DI f32x16 mfma(u32x4 a, u32x4 b, f32x16 c) {
    return __builtin_amdgcn_mfma_f32_32x32x16_bf16(__builtin_bit_cast(bf16x8, a), __builtin_bit_cast(bf16x8, b), c, 0, 0, 0);
}
DI f32x4 mfma16(u32x4 a, u32x4 b, f32x4 c) {
    return __builtin_amdgcn_mfma_f32_16x16x32_bf16(__builtin_bit_cast(bf16x8, a), __builtin_bit_cast(bf16x8, b), c, 0, 0, 0);
}
DI float fsin(float rev) { return __builtin_amdgcn_sinf(rev); }
DI float fcos(float rev) { return __builtin_amdgcn_cosf(rev); }
DI unsigned cs(float rev) { return pk(fcos(rev), fsin(rev)); }
DI u32x4 trread2(const char* p0, const char* p1) {
    s16x4 a = __builtin_amdgcn_ds_read_tr16_b64_v4i16((lds_s16x4p)p0);
    s16x4 b = __builtin_amdgcn_ds_read_tr16_b64_v4i16((lds_s16x4p)p1);
    return __builtin_bit_cast(u32x4, __builtin_shufflevector(a, b, 0, 1, 2, 3, 4, 5, 6, 7));
}
DI u32x4 pack8(const f32x16& x, int o) {
    u32x4 r = {pk(x[o], x[o + 1]), pk(x[o + 2], x[o + 3]), pk(x[o + 4], x[o + 5]), pk(x[o + 6], x[o + 7])};
    return r;
}
DI f32x16 zero16() { f32x16 z = {0.f, 0.f, 0.f, 0.f, 0.f, 0.f, 0.f, 0.f, 0.f, 0.f, 0.f, 0.f, 0.f, 0.f, 0.f, 0.f}; return z; }
DI f32x4 zero4() { f32x4 z = {0.f, 0.f, 0.f, 0.f}; return z; }
#define PIN(x) asm volatile("" : "+v"(x))
#define DRAIN() asm volatile("s_waitcnt vmcnt(0)" ::: "memory")
#define BAR() do { asm volatile("s_waitcnt lgkmcnt(0)" ::: "memory"); __builtin_amdgcn_s_barrier(); asm volatile("" ::: "memory"); } while (0)
#define ST_AUX 16
#define LD_AUX 16
#define LDB_AUX 16
#define DLY_H 10
DI void st16(rsrc_t rs, unsigned off, unsigned so, u32x4 v) { __builtin_amdgcn_raw_buffer_store_b128(v, rs, off, so, ST_AUX); }
DI u32x4 ld16(rsrc_t rs, unsigned off, unsigned so) { return __builtin_amdgcn_raw_buffer_load_b128(rs, off, so, LD_AUX); }
DI u32x4 ld16b(rsrc_t rs, unsigned off, unsigned so) { return __builtin_amdgcn_raw_buffer_load_b128(rs, off, so, LDB_AUX); }
DI unsigned ld4(rsrc_t rs, unsigned off, unsigned so) { return __builtin_amdgcn_raw_buffer_load_b32(rs, off, so, 16); }
DI void st4(rsrc_t rs, unsigned off, unsigned so, unsigned v) { __builtin_amdgcn_raw_buffer_store_b32(v, rs, off, so, 16); }
DI unsigned xor3(unsigned a, unsigned b, unsigned c) { return __builtin_amdgcn_bitop3_b32(a, b, c, 0x96); }
DI u32x4 piece(unsigned a, unsigned b, unsigned c, unsigned tag) { u32x4 v = {a, b, c, xor3(a, b, c) ^ tag}; return v; }
DI f32x4 asf(u32x4 v) { return __builtin_bit_cast(f32x4, v); }
DI f32x4 sub4(const f32x16& x, int o) { f32x4 r = {x[o], x[o + 1], x[o + 2], x[o + 3]}; return r; }
DI u32x4 pk44(f32x4 a, f32x4 b) { u32x4 r = {pk(a[0], a[1]), pk(a[2], a[3]), pk(b[0], b[1]), pk(b[2], b[3])}; return r; }
DI u32x4 pkc(f32x4 re, f32x4 im) { u32x4 r = {pk(re[0], im[0]), pk(re[1], im[1]), pk(re[2], im[2]), pk(re[3], im[3])}; return r; }
DI rsrc_t mkrs(const void* p, unsigned bytes) { return __builtin_amdgcn_make_buffer_rsrc((void*)p, 0, bytes, 0x00020000); }
template <int NT>
DI void dma16(const void* g, unsigned lds) {
    unsigned keep;
    if (NT) asm volatile("s_mov_b32 %0, m0\n\ts_mov_b32 m0, %2\n\ts_nop 0\n\tglobal_load_lds_dwordx4 %1, off nt\n\ts_mov_b32 m0, %0" : "=&s"(keep) : "v"(g), "s"(lds) : "memory");
    else asm volatile("s_mov_b32 %0, m0\n\ts_mov_b32 m0, %2\n\ts_nop 0\n\tglobal_load_lds_dwordx4 %1, off\n\ts_mov_b32 m0, %0" : "=&s"(keep) : "v"(g), "s"(lds) : "memory");
}
#define OUTB_AUX 0

DI void fetch4(rsrc_t rs, unsigned off, unsigned stride, unsigned tag, u32x4 (&f)[3]) {
    u32x4 x[4];
#pragma unroll 1
    for (unsigned spins = 0; spins < SPIN_CAP; ++spins) {
        asm volatile("" ::: "memory");
        unsigned c[4];
#pragma unroll
        for (int t = 0; t < 4; ++t) x[t] = ld16(rs, off, t * stride);
#pragma unroll
        for (int t = 0; t < 4; ++t) c[t] = xor3(xor3(x[t][0], x[t][1], x[t][2]), x[t][3], tag);
        if (__builtin_amdgcn_ballot_w64((c[0] | c[1] | c[2] | c[3]) != 0) == 0) break;
    }
#pragma unroll
    for (int e = 0; e < 3; ++e) f[e] = u32x4{x[0][e], x[1][e], x[2][e], x[3][e]};
}

template <int CTRL, int BANK>
DI float dppo(float old, float v) {
    return __int_as_float(__builtin_amdgcn_update_dpp(__float_as_int(old), __float_as_int(v), CTRL, 0xf, BANK, false));
}
template <int CTRL>
DI float dppz(float v) { return __int_as_float(__builtin_amdgcn_update_dpp(0, __float_as_int(v), CTRL, 0xf, 0xf, true)); }
DI void reduce_tile(const f32x16& a, f32x4& o, float& c) {
    float r0 = a[0], r1 = a[1], r2 = a[2], r3 = a[3], r4 = a[4], r5 = a[5], r6 = a[6], r7 = a[7];
    float r8 = a[8], r9 = a[9], r10 = a[10], r11 = a[11], r12 = a[12], r13 = a[13], r14 = a[14], r15 = a[15];
    asm volatile(
        "s_nop 7\n\t"
        "s_nop 3\n\t"
        "v_mov_b32 %16, 0\n\t"
        "v_add_f32_dpp %0, %1, %0 row_shr:4 row_mask:0xf bank_mask:0xa\n\t"
        "v_add_f32_dpp %2, %3, %2 row_shr:4 row_mask:0xf bank_mask:0xa\n\t"
        "v_add_f32_dpp %4, %5, %4 row_shr:4 row_mask:0xf bank_mask:0xa\n\t"
        "v_add_f32_dpp %6, %7, %6 row_shr:4 row_mask:0xf bank_mask:0xa\n\t"
        "v_add_f32_dpp %8, %9, %8 row_shr:4 row_mask:0xf bank_mask:0xa\n\t"
        "v_add_f32_dpp %10, %11, %10 row_shr:4 row_mask:0xf bank_mask:0xa\n\t"
        "v_add_f32_dpp %12, %13, %12 row_shr:4 row_mask:0xf bank_mask:0xa\n\t"
        "v_add_f32_dpp %14, %15, %14 row_shr:4 row_mask:0xf bank_mask:0xa\n\t"
        "v_mov_b32_dpp %16, %15 row_shl:4 row_mask:0xf bank_mask:0x5\n\t"
        "v_add_f32_dpp %2, %1, %2 row_shl:4 row_mask:0xf bank_mask:0x5\n\t"
        "v_add_f32_dpp %6, %5, %6 row_shl:4 row_mask:0xf bank_mask:0x5\n\t"
        "v_add_f32_dpp %10, %9, %10 row_shl:4 row_mask:0xf bank_mask:0x5\n\t"
        "v_add_f32_dpp %14, %13, %14 row_shl:4 row_mask:0xf bank_mask:0x5\n\t"
        "v_add_f32_dpp %4, %3, %4 row_shl:4 row_mask:0xf bank_mask:0x5\n\t"
        "v_add_f32_dpp %8, %7, %8 row_shl:4 row_mask:0xf bank_mask:0x5\n\t"
        "v_add_f32_dpp %12, %11, %12 row_shl:4 row_mask:0xf bank_mask:0x5\n\t"
        "v_add_f32_dpp %0, %2, %0 row_ror:8 row_mask:0xf bank_mask:0xc\n\t"
        "v_add_f32_dpp %4, %6, %4 row_ror:8 row_mask:0xf bank_mask:0xc\n\t"
        "v_add_f32_dpp %8, %10, %8 row_ror:8 row_mask:0xf bank_mask:0xc\n\t"
        "v_add_f32_dpp %12, %14, %12 row_ror:8 row_mask:0xf bank_mask:0xc\n\t"
        "v_add_f32_dpp %16, %14, %16 row_ror:8 row_mask:0xf bank_mask:0x3\n\t"
        "v_add_f32_dpp %4, %2, %4 row_ror:8 row_mask:0xf bank_mask:0x3\n\t"
        "v_add_f32_dpp %8, %6, %8 row_ror:8 row_mask:0xf bank_mask:0x3\n\t"
        "v_add_f32_dpp %12, %10, %12 row_ror:8 row_mask:0xf bank_mask:0x3\n\t"
        : "+v"(r0), "+v"(r1), "+v"(r2), "+v"(r3), "+v"(r4), "+v"(r5), "+v"(r6), "+v"(r7), "+v"(r8), "+v"(r9), "+v"(r10), "+v"(r11),
          "+v"(r12), "+v"(r13), "+v"(r14), "+v"(r15), "=&v"(c));
    o = f32x4{r0, r4, r8, r12};
}

#define CODE_WGS 0
#define CODE_BYTES 21248
#define OUT_AUX 0

template <int V> struct IC { static constexpr int v = V; };
struct ConvCtx {
    int pa, pb, bz, pu, csrc, yo;
    float dv;
    bool first, diag;
};
DI float edge_sum(f32x4 d, bool diag) {
    float t = dppz<0x104>(d[3]) + dppz<0x108>(d[2]) + dppz<0x10c>(d[1]);
    t = diag ? t : 0.f;
    t = __builtin_amdgcn_mfma_f32_16x16x4f32(1.0f, t, zero4(), 0, 0, 0)[0];
    t += dppz<0xb1>(t);
    t += dppz<0x4e>(t);
    return t;
}
DI u32x2 finish_tile(const f32x16& acc, float edge, u32x2 u, const ConvCtx& c) {
    f32x4 o;
    float cy;
    reduce_tile(acc, o, cy);
    cy = __int_as_float(__builtin_amdgcn_ds_bpermute(c.csrc, __float_as_int(cy)));
    const f32x4 uf = {bfhi(u[1]), bflo(u[1]), bfhi(u[0]), bflo(u[0])};
    o = uf * c.dv + o;
    o[0] += c.first ? edge : cy;
    return u32x2{pk(o[0], o[1]), pk(o[2], o[3])};
}
template <int PART>
DI void conv(char* smem, const ConvCtx& c, const u32x4 (&ea)[4], u32x4 a0) {
    constexpr int T1 = 7 - PART, T2 = PART == 0 ? 3 : 2 * PART, T3 = 2 - PART;
    constexpr int NS = PART == 2 ? 24 : 16, PF = PART == 2 ? 2 : 1;
    constexpr int F2 = 4 * T2 + 4 + PF, F3 = PART == 0 ? -9 : (PART == 1 ? 8 + PF : NS + PF);
    const char* pa = smem + c.pa;
    const char* pb = smem + c.pb;
    u32x4 fa[32], fb[32], fc[8];
    f32x16 acc1 = zero16(), acc2 = zero16(), acc3 = zero16();
    u32x4 eb[7];
    u32x2 u1, u2, u3, y2;
    f32x4 d1 = zero4(), d2 = zero4(), d3 = zero4();
    float e1, e2, e3 = 0.f;
    fa[0] = a0;
#define LDA(m) fa[m] = trread2(pa + 128 * (m), pa + 128 * (m) + 32)
#define BFR(n) (*(const u32x4*)(pb + 128 * ((n) + 3)))
#define LDB(n) fb[(n) + 3] = BFR(n)
#define EB(T, i) (*(const u32x4*)(smem + c.bz + 512 * (T) - 1024 * (i)))
#pragma unroll
    for (int i = 0; 2 * i < T1; ++i) eb[i] = EB(T1, i);
#pragma unroll
    for (int i = 0; 2 * i < T2; ++i) eb[4 + i] = EB(T2, i);
    if (PART == 1) eb[6] = EB(1, 0);
#pragma unroll
    for (int s = 0; s < NS + PF; ++s) {
        asm volatile("" ::: "memory");
        if (s < NS) {
            if (s >= 1) LDA(s);
            if (PART == 0) { LDB(28 - s); LDB(12 - s); LDA(s + 16); }
            if (PART == 1) { LDB(24 - s); if (s < 12) { LDB(8 - s); LDA(s + 16); } }
            if (PART == 2) { if (s < 4) LDB(20 - s); if (s < 20) LDB(16 - s); }
            if (PART == 1 && s < 8) fc[s] = BFR(4 - s);
            if (PART == 2 && s >= 20) fc[s - 20] = trread2(pa + 128 * (s - 20), pa + 128 * (s - 20) + 32);
        }
        if (s == F3 - 1) u3 = *(const u32x2*)(smem + c.pu + 512 * T3);
        if (s == F2 - 1) u2 = *(const u32x2*)(smem + c.pu + 512 * T2);
        if (s == NS + PF - 1) u1 = *(const u32x2*)(smem + c.pu + 512 * T1);
        __builtin_amdgcn_sched_barrier(0);
        if (s == 0) {
#pragma unroll
            for (int i = 0; 2 * i < T1; ++i) {
                d1 = mfma16(ea[i], eb[i], d1);
                if (2 * i < T2) d2 = mfma16(ea[i], eb[4 + i], d2);
            }
            if (PART == 1) d3 = mfma16(ea[0], eb[6], d3);
        }
        if (s >= PF) {
            const int m = s - PF;
            acc1 = mfma(fa[m], fb[4 * T1 - m + 3], acc1);
            if (m < 4 * T2 + 4) acc2 = mfma(fa[m], fb[4 * T2 - m + 3], acc2);
            if (PART < 2 && m < 4 * T2 + 4) acc1 = mfma(fa[m + 16], fb[4 * T2 - m + 3], acc1);
            if (PART == 1 && m < 8) acc3 = mfma(fa[m], fc[m], acc3);
            if (PART == 2 && m >= 20) acc3 = mfma(fc[m - 20], fb[20 - m + 3], acc3);
        }
        if (s == PF) {
            e1 = edge_sum(d1, c.diag);
            e2 = edge_sum(d2, c.diag);
            if (PART == 1) e3 = edge_sum(d3, c.diag);
        }
        if (s == F3) *(u32x2*)(smem + c.yo + (LB_U + 3584 - LB_Y)) = finish_tile(acc3, e3, u3, c);
        if (s == F2) y2 = finish_tile(acc2, e2, u2, c);
        __builtin_amdgcn_sched_barrier(0);
    }
#undef LDA
#undef LDB
#undef BFR
#undef EB
    if (PART == 0) y2 = finish_tile(acc2, e2, u2, c);
    if (PART == 2) *(u32x2*)(smem + c.yo + (LB_U + 3584 - LB_Y)) = finish_tile(acc3, e3, u3, c);
    const u32x2 y1 = finish_tile(acc1, e1, u1, c);
    *(u32x2*)(smem + c.yo + 512) = y2;
    *(u32x2*)(smem + c.yo) = y1;
}

constexpr int LA_EX = 10240, LA_E1 = 28672, LA_T3 = 53248, LA_T1 = 67584;

__global__ __launch_bounds__(512, 4) void k_fused(const u16* __restrict__ x, const u16* __restrict__ wr, const u16* __restrict__ wi,
                                                  const u16* __restrict__ qq, const u16* __restrict__ kk, const u16* __restrict__ vv,
                                                  const u16* __restrict__ shk, const u16* __restrict__ hk, const u16* __restrict__ dd,
                                                  u16* __restrict__ out, char* ws) {
    __shared__ __attribute__((aligned(16))) char smem[LDS_BYTES];
    const int tid = threadIdx.x, lane = tid & 63;
    const int w = __builtin_amdgcn_readfirstlane(tid >> 6);
    const int n = lane & 31, hh = lane >> 5;
    const int g = lane >> 4, q = (lane >> 2) & 3, p = lane & 3;
    const unsigned lb = (unsigned)(size_t)(lds_vp)smem;
    STAMP_DECL
    STAMP(0);
    unsigned warm = 0;
    if (w >= 5 && blockIdx.x < CODE_WGS) {
        unsigned long long pc;
        asm volatile("s_getpc_b64 %0" : "=s"(pc));
        asm volatile("global_load_dword %0, %1, %2" : "=v"(warm) : "v"(min((tid - 320) * 128, CODE_BYTES)), "s"(pc) : "memory");
    }
    asm volatile("" :: "s"(x), "s"(wr), "s"(wi), "s"(qq), "s"(kk), "s"(vv), "s"(shk), "s"(hk), "s"(dd), "s"(out), "s"(ws));
    const rsrc_t rs = mkrs(ws, WS_END);
#define rs0 rs
    if (blockIdx.x < 256) {
        const int i2 = blockIdx.x;
        const bool hasU = i2 < 8 * 27;
        if (w < 4) {
            {
                const char* r0 = (const char*)(x + (size_t)(256 * (2 * w) + i2) * D_);
                dma16<NT_X>(r0 + lane * 16, lb + w * 2560);
                dma16<NT_X>(r0 + 256 * D_ * 2 + lane * 16, lb + w * 2560 + 1024);
                if (lane < 32) dma16<NT_X>(r0 + g * (256 * D_ * 2) + 1024 + (lane & 15) * 16, lb + w * 2560 + 2048);
            }
            unsigned ep = ld4(rs0, WS_EP, 0);
            u32x4 c1m[2];
#pragma unroll
            for (int t = 0; t < 4; ++t) {
                unsigned f = cs((float)((256 * (4 * hh + t) + i2) * (n >> 2)) * (1.0f / 2048.0f) + 0.25f * (n & 1));
                c1m[0][t] = (n & 2) ? 0u : f;
                c1m[1][t] = (n & 2) ? f : 0u;
            }
            const int pi = min(28 * w + n, PK - 1);
            int exr[3];
#pragma unroll
            for (int e = 0; e < 3; ++e) {
                int kd = min(3 * pi + e, 320);
                int m = (kd * 1639) >> 16, c = kd - 40 * m;
                int sa = 21 * m + c, sb = 355 - sa;
                exr[e] = LA_EX + (c <= 20 ? sa : sb) * 48 + 16 * hh;
                PIN(exr[e]);
            }
            PIN(c1m[0]); PIN(c1m[1]);
            int xao = w * 2560 + (g & 1) * 1024 + (8 * hh + q) * 32 + p * 8;
            int xto = w * 2560 + 2048 + (g & 1) * 256 + q * 32 + p * 8;
            int exw = LA_EX + (42 * hh + n) * 48 + 8 * w;
            int l16 = lane * 16 + 32768;
            unsigned rowoff = i2 * ROWB + pi * 16;
            unsigned x1o = (n < 28 && 28 * w + n < PK) ? WS_X1 + (i2 >> 1) * (2 * ROWB) + (i2 & 1) * 64 + (pi >> 2) * 128 + (pi & 3) * 16 + hh * (PK * 32) : 0x80000000u;
            PIN(xao); PIN(xto); PIN(exw); PIN(x1o); PIN(l16);
            STAMP(1);
            __builtin_amdgcn_s_setprio(2);
            PIN(ep);
            const unsigned tag = 2u * __builtin_amdgcn_readfirstlane(ep) + 3u;
            DRAIN();
            BAR();
            STAMP(2);
            {
                const char* xa = smem + xao;
                const char* xt = smem + xto;
                const char* tp = smem + l16 + (LA_T1 - 32768);
                u32x4 tb[12];
                u32x4 a0 = trread2(xa, xa + 128);
                tb[0] = *(const u32x4*)tp; tb[1] = *(const u32x4*)(tp + 1024);
                u32x4 a1 = trread2(xa + 512, xa + 640);
                tb[2] = *(const u32x4*)(tp + 2048); tb[3] = *(const u32x4*)(tp + 3072);
                u32x4 a2 = trread2(xt, xt + 128);
                tb[4] = *(const u32x4*)(tp + 4096); tb[5] = *(const u32x4*)(tp + 5120);
                __builtin_amdgcn_sched_barrier(0);
                f32x16 tr = mfma(a0, tb[0], zero16());
                f32x16 ti = mfma(a0, tb[1], zero16());
                __builtin_amdgcn_sched_barrier(0);
#pragma unroll
                for (int f = 11; f >= 6; --f) tb[f] = *(const u32x4*)(tp + f * 1024);
                __builtin_amdgcn_sched_barrier(0);
                tr = mfma(a1, tb[2], tr);
                ti = mfma(a1, tb[3], ti);
                tr = mfma(a2, tb[4], tr);
                ti = mfma(a2, tb[5], ti);
                f32x16 y[2];
#pragma unroll
                for (int ss = 0; ss < 2; ++ss) {
                    f32x4 r0 = sub4(tr, 8 * ss), r1 = sub4(tr, 8 * ss + 4), i0 = sub4(ti, 8 * ss), i1 = sub4(ti, 8 * ss + 4);
                    f32x4 m2 = r0 * asf(tb[10]), m3 = r1 * asf(tb[11]), m0 = i0 * asf(tb[10]), m1 = i1 * asf(tb[11]);
                    __builtin_amdgcn_sched_barrier(0);
                    m0 = r0 * asf(tb[8]) + m0; m1 = r1 * asf(tb[9]) + m1;
                    m2 = i0 * asf(tb[8]) - m2; m3 = i1 * asf(tb[9]) - m3;
                    __builtin_amdgcn_sched_barrier(0);
                    u32x4 pr = pk44(m0, m1), pi_ = pk44(m2, m3);
                    __builtin_amdgcn_sched_barrier(0);
                    y[ss] = mfma(tb[6], pr, zero16());
                    y[ss] = mfma(tb[7], pi_, y[ss]);
                }
                if (n <= 20) {
#pragma unroll
                    for (int t = 0; t < 8; ++t) {
                        u32x2 v = {pk(y[0][2 * t], y[0][2 * t + 1]), pk(y[1][2 * t], y[1][2 * t + 1])};
                        *(u32x2*)(smem + exw + 21 * ((t & 1) + 4 * (t >> 1)) * 48) = v;
                    }
                }
            }
            BAR();
            {
                const u32x4 b0 = *(const u32x4*)(smem + exr[0]), b2 = *(const u32x4*)(smem + exr[2]), b1 = *(const u32x4*)(smem + exr[1]);
                __builtin_amdgcn_sched_barrier(0);
                f32x16 o0 = mfma(c1m[0], b0, zero16());
                f32x16 o1 = mfma(c1m[0], b2, zero16());
                o0 = mfma(c1m[1], b1, o0);
                u32x4 pc[4];
#pragma unroll
                for (int gq = 0; gq < 4; ++gq)
                    pc[gq] = piece(pk(o0[4 * gq], o0[4 * gq + 1]), pk(o0[4 * gq + 2], o0[4 * gq + 3]), pk(o1[4 * gq], o1[4 * gq + 1]), tag);
#pragma unroll
                for (int gq = 0; gq < 4; ++gq) st16(rs, x1o, gq * 2 * PK * 32, pc[gq]);
            }
            STAMP(3);
            DRAIN();
            STAMP(4);
            BAR();
            u32x4 tc[14];
#pragma unroll
            for (int f = 0; f < 14; ++f) tc[f] = *(const u32x4*)(smem + l16 + (LA_T3 - 32768) + f * 1024);
            u32x4 ci[2];
#pragma unroll
            for (int t = 0; t < 4; ++t) {
                unsigned f = cs(0.25f * (n & 1) - (float)((256 * (n >> 2) + i2) * (4 * hh + t)) * (1.0f / 2048.0f));
                ci[0][t] = (n & 2) ? 0u : f;
                ci[1][t] = (n & 2) ? f : 0u;
            }
            PIN(ci[0]); PIN(ci[1]);
#pragma unroll
            for (int f = 0; f < 14; ++f) PIN(tc[f]);
            unsigned zoff = WS_Z1 + rowoff + 4 * hh * (PK * 16);
            const int k1c = min(n, 20);
            int e3o = (hh * 324 + 3 * pi) * 4, s0o = (2 * w * 324 + 160 * hh + k1c) * 4, s1o = (2 * w * 324 + 200 - 160 * hh - k1c) * 4;
            unsigned oo = ((256 * 2 * w + i2) * (2 * D_) + 16 * n + 8 * hh) * 2;
            unsigned oo2 = n < 8 ? oo + 1024 : 0x80000000u;
            PIN(zoff); PIN(e3o); PIN(s0o); PIN(s1o); PIN(oo); PIN(oo2);
            const rsrc_t ro = mkrs(out, S_ * 2 * D_ * 2);
            if (hasU) {
                BAR(); BAR(); BAR();
            } else {
#pragma unroll 1
                for (unsigned spins = 0; spins < SPIN_CAP; ++spins) {
                    __builtin_amdgcn_s_sleep(4);
                    asm volatile("" ::: "memory");
                    u32x4 v = ld16(rs, WS_Z1 + i2 * ROWB + w * 2 * PK * 16, 0);
                    if (__builtin_amdgcn_readfirstlane(v[0] ^ v[1] ^ v[2] ^ v[3]) == tag) break;
                }
            }
            STAMP(9);
            {
                u32x4 zb[3];
                fetch4(rs, zoff, PK * 16, tag, zb);
                STAMP(10);
                f32x16 o0 = mfma(ci[0], zb[0], zero16());
                f32x16 o1 = mfma(ci[0], zb[2], zero16());
                o0 = mfma(ci[1], zb[1], o0);
                unsigned* e3 = (unsigned*)(smem + e3o);
#pragma unroll
                for (int gq = 0; gq < 4; ++gq) {
                    e3[gq * 648] = pk(o0[4 * gq], o0[4 * gq + 1]);
                    e3[gq * 648 + 1] = pk(o0[4 * gq + 2], o0[4 * gq + 3]);
                    e3[gq * 648 + 2] = pk(o1[4 * gq], o1[4 * gq + 1]);
                }
            }
            BAR();
            if (i2 == 0 && tid == 0) st4(rs0, WS_EP, 0, ep + 1u);
            {
                const unsigned* s0 = (const unsigned*)(smem + s0o);
                const unsigned* s1 = (const unsigned*)(smem + s1o);
                const u32x4 f0 = {s0[0], s0[40], s0[80], s0[120]}, f1 = {s0[324], s0[364], s0[404], s0[444]};
                const u32x4 f2 = {s1[120], s1[80], s1[40], s1[0]}, f3 = {s1[444], s1[404], s1[364], s1[324]};
                __builtin_amdgcn_sched_barrier(0);
                f32x16 tr = mfma(f0, tc[0], zero16());
                f32x16 ti = mfma(f1, tc[0], zero16());
                tr = mfma(f2, tc[1], tr);
                ti = mfma(f3, tc[1], ti);
                f32x4 re[3], im[3], mr[3], mi[3];
#pragma unroll
                for (int r = 0; r < 12; ++r) {
                    auto sw = __builtin_amdgcn_permlane16_swap(__float_as_uint(tr[r]), __float_as_uint(ti[r]), false, false);
                    re[r >> 2][r & 3] = __uint_as_float(sw[0]);
                    im[r >> 2][r & 3] = __uint_as_float(sw[1]);
                }
                __builtin_amdgcn_sched_barrier(0);
#pragma unroll
                for (int j = 0; j < 3; ++j) { mr[j] = im[j] * asf(tc[11 + j]); mi[j] = re[j] * asf(tc[11 + j]); }
                __builtin_amdgcn_sched_barrier(0);
#pragma unroll
                for (int j = 0; j < 3; ++j) { mr[j] = re[j] * asf(tc[8 + j]) - mr[j]; mi[j] = im[j] * asf(tc[8 + j]) + mi[j]; }
                __builtin_amdgcn_sched_barrier(0);
                const u32x4 a0 = pk44(mr[0], mr[1]), a1 = pk44(mi[0], mi[1]), a2 = pk44(mr[2], mi[2]);
                f32x16 y0 = mfma(a0, tc[2], zero16());
                y0 = mfma(a1, tc[3], y0);
                y0 = mfma(a2, tc[4], y0);
                f32x16 y1 = mfma(a0, tc[5], zero16());
                y1 = mfma(a1, tc[6], y1);
                y1 = mfma(a2, tc[7], y1);
                __builtin_amdgcn_raw_buffer_store_b128(pack8(y0, 0), ro, oo, 0, OUT_AUX);
                __builtin_amdgcn_raw_buffer_store_b128(pack8(y0, 8), ro, oo, 256 * 2 * D_ * 2, OUT_AUX);
                __builtin_amdgcn_raw_buffer_store_b128(pack8(y1, 0), ro, oo2, 0, OUT_AUX);
                __builtin_amdgcn_raw_buffer_store_b128(pack8(y1, 8), ro, oo2, 256 * 2 * D_ * 2, OUT_AUX);
            }
            STAMP(11);
        } else {
            const int w2 = w - 4;
#pragma unroll
            for (int k = 0; k < 3; ++k) dma16<0>((const char*)g_t1.v + (3 * w2 + k) * 1024 + lane * 16, lb + LA_T1 + (3 * w2 + k) * 1024);
            const unsigned ep = ld4(rs0, WS_EP, 0);
            const int k1s = i2 & 7, pi = 4 * (i2 >> 3) + p;
            const unsigned we = (4 * w2 + q + 64 * g) * NW + k1s * DF + 3 * pi;
            unsigned vo = hasU ? (we * 2) & ~3u : 0x80000000u;
            PIN(vo);
            u32x4 fre, fim, fin;
            f32x4 tc, ts;
#pragma unroll
            for (int t = 0; t < 4; ++t) {
                float an = (float)((lane & 15) * (4 * g + t)) * (1.0f / 16.0f);
                float c = fcos(an), s = fsin(an);
                fre[t] = pk(c, s);
                fim[t] = pk(-s, c);
                fin[t] = fim[t] ^ 0x80008000u;
                float b = (float)((4 * w2 + q) * (4 * g + t)) * (1.0f / 256.0f);
                tc[t] = fcos(b); ts[t] = fsin(b);
            }
            PIN(fre); PIN(fim); PIN(fin); PIN(tc); PIN(ts);
            DRAIN();
            BAR();
            asm volatile("" :: "v"(warm));
            u32x2 wraw[4][2];
            {
                const rsrc_t rwr = mkrs(wr, S_ * DF * 2), rwi = mkrs(wi, S_ * DF * 2);
#pragma unroll
                for (int r = 0; r < 4; ++r) {
                    wraw[r][0] = __builtin_amdgcn_raw_buffer_load_b64(rwr, vo, r * 16 * NW * 2, NT_W);
                    wraw[r][1] = __builtin_amdgcn_raw_buffer_load_b64(rwi, vo, r * 16 * NW * 2, NT_W);
                }
            }
#pragma unroll
            for (int k = 0; k < 4; ++k) {
                const int f = min(4 * w2 + k, 13);
                dma16<0>((const char*)g_t3.v + f * 1024 + lane * 16, lb + LA_T3 + f * 1024);
            }
            BAR();
            DRAIN();
            f32x4 wf[2][3];
            {
                const unsigned sh = (we & 1) * 16;
#pragma unroll
                for (int r = 0; r < 4; ++r)
#pragma unroll
                    for (int a = 0; a < 2; ++a) {
                        unsigned lo = __builtin_amdgcn_alignbit(wraw[r][a][1], wraw[r][a][0], sh), hi = wraw[r][a][1] >> sh;
                        wf[a][0][r] = bflo(lo); wf[a][1][r] = bfhi(lo); wf[a][2][r] = bflo(hi);
                    }
#pragma unroll
                for (int e = 0; e < 3; ++e) { PIN(wf[0][e]); PIN(wf[1][e]); }
            }
            const unsigned tag = 2u * __builtin_amdgcn_readfirstlane(ep) + 3u;
            unsigned coloff = (64 * g + 4 * w2 + q) * ROWB + (k1s * PK + pi) * 16;
            int ewo = LA_E1 + (g * 768 + (16 * w2 + p) * 4 + q) * 4, ero = LA_E1 + w2 * 3072 + lane * 16;
            unsigned xoff = WS_X1 + (64 * g + 4 * w2 + (q & 2)) * ROWB + (k1s * 27 + (i2 >> 3)) * 128 + (lane & 7) * 16;
            PIN(coloff); PIN(ewo); PIN(ero); PIN(xoff);
            unsigned* ew = (unsigned*)(smem + ewo);
            const char* er = smem + ero;
            STAMP(1);
            __builtin_amdgcn_s_setprio(2);
            BAR();
            if (hasU) {
            STAMP(5);
            u32x4 f[3];
            f32x4 re[3], im[3], mr[3], mi[3];
            fetch4(rs, xoff, 16 * ROWB, tag, f);
            STAMP(6);
#define DFT16(FI) _Pragma("unroll") for (int e = 0; e < 3; ++e) { re[e] = mfma16(fre, f[e], zero4()); im[e] = mfma16(FI, f[e], zero4()); }
#define TWID_STORE(OFF) \
            _Pragma("unroll") for (int e = 0; e < 3; ++e) { \
                mr[e] = im[e] * ts; mi[e] = re[e] * ts; \
                mr[e] = re[e] * tc + mr[e]; mi[e] = im[e] * tc - mi[e]; \
                u32x4 v = pkc(mr[e], mi[e]); \
                _Pragma("unroll") for (int r = 0; r < 4; ++r) ew[OFF + e * 256 + r * 16] = v[r]; \
            }
            DFT16(fim);
            __builtin_amdgcn_sched_barrier(0);
            TWID_STORE(0);
            BAR();
#pragma unroll
            for (int e = 0; e < 3; ++e) f[e] = *(const u32x4*)(er + e * 1024);
            __builtin_amdgcn_sched_barrier(0);
            DFT16(fin);
            __builtin_amdgcn_sched_barrier(0);
#pragma unroll
            for (int e = 0; e < 3; ++e) {
                mr[e] = im[e] * wf[1][e]; mi[e] = re[e] * wf[1][e];
                mr[e] = re[e] * wf[0][e] + mr[e]; mi[e] = im[e] * wf[0][e] - mi[e];
                f[e] = pkc(mr[e], mi[e]);
                re[e] = mfma16(fre, f[e], zero4()); im[e] = mfma16(fim, f[e], zero4());
            }
            __builtin_amdgcn_sched_barrier(0);
            TWID_STORE(3072);
            BAR();
#pragma unroll
            for (int e = 0; e < 3; ++e) f[e] = *(const u32x4*)(er + 12288 + e * 1024);
            __builtin_amdgcn_sched_barrier(0);
            DFT16(fin);
            __builtin_amdgcn_sched_barrier(0);
#pragma unroll
            for (int e = 0; e < 3; ++e) f[e] = pkc(re[e], im[e]);
#pragma unroll
            for (int r = 0; r < 4; ++r) st16(rs, coloff, WS_Z1 + r * 16 * ROWB, piece(f[0][r], f[1][r], f[2][r], tag));
            STAMP(7);
            DRAIN();
            STAMP(8);
            BAR();
            }
        }
    } else {
        const int bb = blockIdx.x - 256;
        const int xg = bb & 7, ib = bb >> 3;
        const bool cons = ib < 30;
        const int gl = min(ib / 3, 9), part = ib - 3 * (ib / 3);
        const int ch0 = 80 * xg + 8 * gl;
        const unsigned epo = WS_EP + 256 * (1 + xg);
        unsigned ep;
        if (w < 5) {
            __builtin_amdgcn_s_setprio(1);
            const int rg = tid / 40, cp = tid - 40 * rg;
            const int chp = 80 * xg + 2 * cp, s0 = 64 * ib + 8 * rg;
            const rsrc_t rk = mkrs(kk, S_ * D_ * 2), rv = mkrs(vv, S_ * D_ * 2);
            const unsigned vo = (s0 * D_ + chp) * 2;
            const unsigned ho = s0 > 0 ? vo - 3 * D_ * 2 : 0x80000000u;
            unsigned kr[11], vr[8];
            const u32x4 sw = *(const u32x4*)(shk + chp * 4);
#pragma unroll
            for (int i = 0; i < 3; ++i) kr[i] = __builtin_amdgcn_raw_buffer_load_b32(rk, ho + i * D_ * 2, 0, NT_KV);
#pragma unroll
            for (int i = 0; i < 8; ++i) {
                kr[3 + i] = __builtin_amdgcn_raw_buffer_load_b32(rk, vo + (i % 3) * D_ * 2, (i / 3) * 3 * D_ * 2, NT_KV);
                vr[i] = __builtin_amdgcn_raw_buffer_load_b32(rv, vo + (i % 3) * D_ * 2, (i / 3) * 3 * D_ * 2, NT_KV);
            }
            ep = ld4(rs, epo, 0);
            __builtin_amdgcn_sched_barrier(0);
            STAMP(1);
            f32x2 wt[4] = {{bflo(sw[0]), bflo(sw[2])}, {bfhi(sw[0]), bfhi(sw[2])}, {bflo(sw[1]), bflo(sw[3])}, {bfhi(sw[1]), bfhi(sw[3])}};
            f32x2 kf[11], o[8];
#pragma unroll
            for (int i = 0; i < 11; ++i) {
                kf[i] = f32x2{bflo(kr[i]), bfhi(kr[i])};
            }
#pragma unroll
            for (int e = 0; e < 8; ++e) {
                f32x2 a = kf[e] * wt[3];
                a = kf[e + 1] * wt[2] + a;
                a = kf[e + 2] * wt[1] + a;
                a = kf[e + 3] * wt[0] + a;
                o[e] = a * f32x2{bflo(vr[e]), bfhi(vr[e])};
            }
#pragma unroll
            for (int c = 0; c < 2; ++c) {
                char* dst = smem + LB_ST + (c * 40 + cp) * 136 + rg * 16;
                *(u32x2*)dst = u32x2{pk(o[0][c], o[1][c]), pk(o[2][c], o[3][c])};
                *(u32x2*)(dst + 8) = u32x2{pk(o[4][c], o[5][c]), pk(o[6][c], o[7][c])};
            }
            PIN(ep);
            STAMP(2);
            BAR();
            const char* src = smem + LB_ST + ((p >> 1) * 40 + 8 * w + g) * 136 + 64 * (p & 1) + 8 * (7 - q);
            const unsigned mo = WS_KV + ((xg * 10 + 2 * w) * 32 + ib) * MSGB;
            const u32x4 u0 = trread2(src, src - 32), u1 = trread2(src + 4 * 136, src + 4 * 136 - 32);
            st16(rs, lane * 16, mo, u0);
            st16(rs, lane * 16, mo + 32 * MSGB, u1);
            STAMP(3);
            DRAIN();
            if (lane < 2) st4(rs, lane * (32 * MSGB) + 1024, mo, 2u * ep + 3u);
            __builtin_amdgcn_s_setprio(0);
            STAMP(4);
        } else {
            ep = ld4(rs, epo, 0);
            const int t3 = tid - 320;
#pragma unroll
            for (int k = 0; k < 2; ++k) {
                const int z = t3 + 192 * k, c = z / 36, o = z - 36 * c;
                if (z < 288) *(u32x4*)(smem + c * CHB + (o < 4 ? o * 16 : LB_Z + (o - 4) * 16)) = u32x4{0u, 0u, 0u, 0u};
            }
            PIN(ep);
            if (cons) {
                __builtin_amdgcn_s_sleep(DLY_H);
                const char* hp = (const char*)(hk + (size_t)ch0 * S_) + lane * 16;
#pragma unroll 1
                for (int k = w - 5; k < 32; k += 3) dma16<NT_H>(hp + k * 1024, lb + (k >> 2) * CHB + LB_H + (k & 3) * 1024);
            }
            BAR();
            DRAIN();
        }
        if (cons) {
            const unsigned tag = 2u * __builtin_amdgcn_readfirstlane(ep) + 3u;
            ConvCtx c;
            {
                const int pi = 2 * (g & 1) + (p >> 1) + 4 * (p & 1);
                const int a = (n & 3) | ((n >> 4) << 2), am = (a + 7) & 7;
                c.pa = w * CHB + 8 + 8 * (8 * hh + q + pi);
                c.pb = w * CHB + LB_U + 64 * (a - hh) + 16 * q - 384;
                c.bz = w * CHB + LB_U + 16 * q - 64 * (1 + 4 * p + g);
                c.pu = w * CHB + LB_U + 64 * a + 16 * q + 8 - 8 * hh;
                c.csrc = 4 * ((hh ? n : ((am & 3) | (q << 2) | ((am >> 2) << 4))) + 32 - 32 * hh);
                c.yo = w * CHB + LB_Y + lane * 8;
                c.dv = bflo(dd[ch0 + w]);
                c.first = (lane & 0x33) == 0;
                c.diag = g == p;
                PIN(c.pa); PIN(c.pb); PIN(c.bz); PIN(c.pu); PIN(c.csrc); PIN(c.yo); PIN(c.dv);
            }
            const int nk = (32 - 4 * part - w + 7) >> 3;
            const unsigned mo = WS_KV + ((xg * 10 + gl) * 32 + w) * MSGB;
            const unsigned m3 = (nk - 1) * 8;
            unsigned rowa, rowb;
            {
                const int T1 = 7 - part, T2 = part == 0 ? 3 : 2 * part, T3 = 2 - part;
                const int lq = (tid >> 2) & 63, nq = lq & 31;
                const int row3 = 32 * ((nq & 3) | ((nq >> 4) << 2)) + 16 * (lq >> 5) + ((nq >> 2) & 3) + 4 * p;
                rowa = (row3 + 256 * (w < 4 ? T1 : T2)) * (D_ * 2);
                rowb = (row3 + ((part > 0 && w < 4) ? 256 * T3 : 0x100000)) * (D_ * 2);
            }
            int uo = (lane >> 3) * CHB + LB_U + w * 128 + (lane & 7) * 16;
            int yso = q * CHB + LB_Y + (w * 64 + g * 16 + p * 4) * 2;
            unsigned fo = min(p, nk - 1) * 8 * MSGB + 1024;
            int bao = w * CHB + LB_H + 8 * (32 * p + 8 * g + q);
            unsigned qoa = rowa, qob = rowb;
            PIN(rowa); PIN(rowb); PIN(uo); PIN(yso); PIN(fo); PIN(bao);
            BAR();
            auto tail = [&](auto part_c) __attribute__((always_inline)) {
            constexpr int PART = decltype(part_c)::v;
            u32x4 ea[4];
#pragma unroll
            for (int i = 0; i < 4; ++i) { ea[i] = trread2(smem + bao + 1024 * i, smem + bao + 1024 * i + 32); PIN(ea[i]); }
            u32x4 a0 = trread2(smem + c.pa, smem + c.pa + 32);
            PIN(a0);
#pragma unroll 1
            for (unsigned spins = 0; spins < SPIN_CAP; ++spins) {
                asm volatile("" ::: "memory");
                unsigned fl = ld4(rs, fo, mo);
                COUNT(13);
                if (__builtin_amdgcn_ballot_w64(fl != tag) == 0) break;
                __builtin_amdgcn_s_sleep(2);
            }
            asm volatile("" ::: "memory");
            STAMP(5);
            {
                u32x4 um[4];
#pragma unroll
                for (int k = 0; k < 4; ++k) um[k] = ld16b(rs, lane * 16, mo + (k < 3 ? 8 * k : m3) * MSGB);
#pragma unroll
                for (int k = 0; k < 3; ++k) *(u32x4*)(smem + uo + 1024 * k) = um[k];
                *(u32x4*)(smem + uo + 128 * m3) = um[3];
            }
            STAMP(6);
            BAR();
            STAMP(9);
            const rsrc_t rq = mkrs(qq, S_ * D_ * 2), ro = mkrs(out, S_ * 2 * D_ * 2);
            const u32x4 qa = __builtin_amdgcn_raw_buffer_load_b128(rq, qoa, ch0 * 2, NT_Q);
            const u32x4 qb = __builtin_amdgcn_raw_buffer_load_b128(rq, qob, ch0 * 2, NT_Q);
            __builtin_amdgcn_sched_barrier(0);
            conv<PART>(smem, c, ea, a0);
            STAMP(7);
            BAR();
            {
                const char* ys = smem + yso;
                const u32x4 y = trread2(ys, ys + 4 * CHB), z = trread2(ys + (LB_U + 3584 - LB_Y), ys + (LB_U + 3584 - LB_Y) + 4 * CHB);
                u32x4 o;
#pragma unroll
                for (int k = 0; k < 4; ++k) o[k] = pk(bflo(y[k]) * bflo(qa[k]), bfhi(y[k]) * bfhi(qa[k]));
                __builtin_amdgcn_raw_buffer_store_b128(o, ro, 2 * rowa, (D_ + ch0) * 2, OUTB_AUX);
#pragma unroll
                for (int k = 0; k < 4; ++k) o[k] = pk(bflo(z[k]) * bflo(qb[k]), bfhi(z[k]) * bfhi(qb[k]));
                __builtin_amdgcn_raw_buffer_store_b128(o, ro, 2 * rowb, (D_ + ch0) * 2, OUTB_AUX);
            }
            };
            if (part == 0) tail(IC<0>{});
            else if (part == 1) tail(IC<1>{});
            else tail(IC<2>{});
            if (ib == 0 && tid == 0) st4(rs, epo, 0, ep + 1u);
            STAMP(8);
        }
    }
    STAMP_OUT;
}
extern "C" void kernel_launch(void* const* d_in, const int* in_sizes, int n_in, void* d_out, int out_size, void* d_ws,
                              size_t ws_size, hipStream_t stream) {
    k_fused<<<512, 512, 0, stream>>>((const u16*)d_in[0], (const u16*)d_in[1], (const u16*)d_in[2], (const u16*)d_in[3],
                                     (const u16*)d_in[4], (const u16*)d_in[5], (const u16*)d_in[6], (const u16*)d_in[7],
                                     (const u16*)d_in[8], (u16*)d_out, (char*)d_ws + WSOFF);
}
```

```cpp
#define DI_ROLE 0
#define DI_IDX 15
#define WSOFF ((ws_size - (16u << 20)) & ~(size_t)0x1fffff)
#define NT_X 0
#define NT_KV 0
#define NT_W 0
#define NT_H 0
#define NT_Q 0
#include <hip/hip_runtime.h>
#include <hip/hip_bf16.h>

typedef __attribute__((ext_vector_type(4))) short s16x4;
typedef __attribute__((ext_vector_type(2))) __bf16 bf16x2;
typedef __attribute__((ext_vector_type(8))) __bf16 bf16x8;
typedef __attribute__((ext_vector_type(2))) float f32x2;
typedef __attribute__((ext_vector_type(4))) float f32x4;
typedef __attribute__((ext_vector_type(16))) float f32x16;
typedef __attribute__((ext_vector_type(2))) unsigned u32x2;
typedef __attribute__((ext_vector_type(4))) unsigned u32x4;
typedef __attribute__((address_space(3))) s16x4* lds_s16x4p;
typedef __attribute__((address_space(3))) void* lds_vp;
typedef unsigned short u16;
typedef __amdgpu_buffer_rsrc_t rsrc_t;

#define DI __device__ __forceinline__

#define STAMP(I) do { if ((I) == 0 || (I) == DI_IDX) { __builtin_amdgcn_sched_barrier(0); asm volatile("s_memrealtime %0\n\ts_waitcnt lgkmcnt(0)" : "=s"(st_[(I) != 0]) :: "memory"); __builtin_amdgcn_sched_barrier(0); } } while (0)
#define STAMP_DECL unsigned long long st_[2] = {0, 0};
#define STAMP_OUT do { if (DI_IDX == 14) { asm volatile("s_waitcnt vmcnt(0)" ::: "memory"); STAMP(14); } const bool sel_ = DI_ROLE == 0 ? (blockIdx.x < 256 && w < 4) : DI_ROLE == 1 ? (blockIdx.x < 256 && w >= 4) : (blockIdx.x >= 256); \
    unsigned long long d_ = (sel_ && st_[1]) ? st_[1] - st_[0] : 0; while (__builtin_amdgcn_s_memrealtime() - st_[0] < 2500 + 2 * d_) __builtin_amdgcn_s_sleep(2); } while (0)
#define COUNT(I)

namespace tab {
constexpr double PI = 3.14159265358979323846;
constexpr double csin(double x) { double t = x, s = x; for (int i = 1; i < 12; ++i) { t *= -x * x / ((2 * i) * (2 * i + 1)); s += t; } return s; }
constexpr double ccos(double x) { double t = 1, s = 1; for (int i = 1; i < 12; ++i) { t *= -x * x / ((2 * i - 1) * (2 * i)); s += t; } return s; }
struct Base { float c[640]; };
constexpr Base mkbase() {
    Base b{};
    for (int i = 0; i < 640; ++i) {
        int m = i > 320 ? 640 - i : i;
        b.c[i] = m <= 160 ? (m <= 80 ? (float)ccos(2 * PI * m / 640) : (float)csin(2 * PI * (160 - m) / 640))
                          : (m >= 240 ? (float)-ccos(2 * PI * (320 - m) / 640) : (float)-csin(2 * PI * (m - 160) / 640));
    }
    return b;
}
constexpr Base base = mkbase();
constexpr float C(int n) { return base.c[((n % 640) + 640) % 640]; }
constexpr float S(int n) { return C(n - 160); }
constexpr unsigned fb(float f) { return __builtin_bit_cast(unsigned, f); }
constexpr unsigned bf(float f) { unsigned u = fb(f); return (u + 0x7fffu + ((u >> 16) & 1u)) >> 16; }
constexpr unsigned p2(float lo, float hi) { return bf(lo) | (bf(hi) << 16); }
struct T1 { unsigned v[12 * 256]; };
struct T3 { unsigned v[14 * 256]; };
constexpr T1 mk1() {
    T1 t{};
    for (int l = 0; l < 64; ++l) {
        const int n = l & 31, hh = l >> 5;
        for (int k = 0; k < 4; ++k) {
            for (int sg = 0; sg < 3; ++sg) {
                const int d1 = 16 * sg + 8 * hh + 2 * k;
                const bool ok = d1 < 40;
                t.v[((2 * sg) * 64 + l) * 4 + k] = ok ? p2(C(16 * d1 * n), C(16 * (d1 + 1) * n)) : 0u;
                t.v[((2 * sg + 1) * 64 + l) * 4 + k] = ok ? p2(-S(16 * d1 * n), -S(16 * (d1 + 1) * n)) : 0u;
            }
            const int k2 = n >> 1, ph = (n & 1) ? (k2 >= 8 ? -160 : 160) : 0;
            const int d2 = 2 * (k & 1) + 8 * (k >> 1) + 4 * hh;
            t.v[(6 * 64 + l) * 4 + k] = p2(C(40 * d2 * k2 + ph), C(40 * (d2 + 1) * k2 + ph));
            t.v[(7 * 64 + l) * 4 + k] = p2(S(40 * d2 * k2 + ph), S(40 * (d2 + 1) * k2 + ph));
            for (int e = 0; e < 2; ++e) {
                t.v[((8 + e) * 64 + l) * 4 + k] = fb(C((k + 8 * e + 4 * hh) * n));
                t.v[((10 + e) * 64 + l) * 4 + k] = fb(S((k + 8 * e + 4 * hh) * n));
            }
        }
    }
    return t;
}
constexpr float wt(int k1) { return k1 > 20 ? 0.f : ((k1 == 0 || k1 == 20) ? 1.0f : 2.0f) * (1.0f / (2048.0f * 640.0f)); }
constexpr T3 mk3() {
    T3 t{};
    for (int l = 0; l < 64; ++l) {
        const int n = l & 31, hh = l >> 5, e = n & 15, ro = n >> 4;
        const int d2 = (e & 3) + 8 * ((e >> 2) & 1) + 4 * (e >> 3);
        for (int k = 0; k < 4; ++k) {
            const int a0 = 160 * ro - 40 * d2 * (4 * hh + k), a1 = 40 * d2 * (8 + 4 * hh + k) - 160 * ro;
            t.v[(0 * 64 + l) * 4 + k] = p2(C(a0), S(a0));
            t.v[(1 * 64 + l) * 4 + k] = p2(C(a1), S(a1));
            for (int tl = 0; tl < 2; ++tl) {
                const int d1 = 32 * tl + n;
                const int ka = 2 * (k & 1) + 8 * (k >> 1) + 4 * hh, kb = 16 + 2 * (k & 1) + 4 * hh;
                t.v[((2 + 3 * tl) * 64 + l) * 4 + k] = p2(wt(ka) * C(16 * d1 * ka), wt(ka + 1) * C(16 * d1 * (ka + 1)));
                t.v[((3 + 3 * tl) * 64 + l) * 4 + k] = p2(-wt(ka) * S(16 * d1 * ka), -wt(ka + 1) * S(16 * d1 * (ka + 1)));
                t.v[((4 + 3 * tl) * 64 + l) * 4 + k] = k < 2 ? p2(wt(kb) * C(16 * d1 * kb), wt(kb + 1) * C(16 * d1 * (kb + 1)))
                                                             : p2(-wt(kb) * S(16 * d1 * kb), -wt(kb + 1) * S(16 * d1 * (kb + 1)));
            }
            for (int j = 0; j < 3; ++j) {
                t.v[((8 + j) * 64 + l) * 4 + k] = fb(C(d2 * (k + 8 * j + 4 * hh)));
                t.v[((11 + j) * 64 + l) * 4 + k] = fb(S(d2 * (k + 8 * j + 4 * hh)));
            }
        }
    }
    return t;
}
}
__device__ __attribute__((aligned(128))) const tab::T1 g_t1 = tab::mk1();
__device__ __attribute__((aligned(128))) const tab::T3 g_t3 = tab::mk3();

constexpr int S_ = 2048, D_ = 640, DF = 321;
constexpr int NW = 8 * DF;
constexpr int PK = 108;
constexpr int ROWB = 8 * PK * 16;
constexpr int MSGB = 1088;
constexpr unsigned WS_X1 = 0, WS_Z1 = 4u << 20, WS_KV = 8u << 20, WS_EP = 12u << 20, WS_END = 13u << 20;
constexpr unsigned SPIN_CAP = 1u << 17;
constexpr int CHB = 8832, LB_H = 64, LB_Y = 2112, LB_Z = 4160, LB_U = 4672, LB_ST = 8 * CHB, LDS_BYTES = LB_ST + 80 * 136;

DI unsigned pk(float lo, float hi) {
    f32x2 v = {lo, hi};
    return __builtin_bit_cast(unsigned, __builtin_convertvector(v, bf16x2));
}
DI float bflo(unsigned u) { return __uint_as_float(u << 16); }
DI float bfhi(unsigned u) { return __uint_as_float(u & 0xffff0000u); }
DI f32x16 mfma(u32x4 a, u32x4 b, f32x16 c) {
    return __builtin_amdgcn_mfma_f32_32x32x16_bf16(__builtin_bit_cast(bf16x8, a), __builtin_bit_cast(bf16x8, b), c, 0, 0, 0);
}
DI f32x4 mfma16(u32x4 a, u32x4 b, f32x4 c) {
    return __builtin_amdgcn_mfma_f32_16x16x32_bf16(__builtin_bit_cast(bf16x8, a), __builtin_bit_cast(bf16x8, b), c, 0, 0, 0);
}
DI float fsin(float rev) { return __builtin_amdgcn_sinf(rev); }
DI float fcos(float rev) { return __builtin_amdgcn_cosf(rev); }
DI unsigned cs(float rev) { return pk(fcos(rev), fsin(rev)); }
DI u32x4 trread2(const char* p0, const char* p1) {
    s16x4 a = __builtin_amdgcn_ds_read_tr16_b64_v4i16((lds_s16x4p)p0);
    s16x4 b = __builtin_amdgcn_ds_read_tr16_b64_v4i16((lds_s16x4p)p1);
    return __builtin_bit_cast(u32x4, __builtin_shufflevector(a, b, 0, 1, 2, 3, 4, 5, 6, 7));
}
DI u32x4 pack8(const f32x16& x, int o) {
    u32x4 r = {pk(x[o], x[o + 1]), pk(x[o + 2], x[o + 3]), pk(x[o + 4], x[o + 5]), pk(x[o + 6], x[o + 7])};
    return r;
}
DI f32x16 zero16() { f32x16 z = {0.f, 0.f, 0.f, 0.f, 0.f, 0.f, 0.f, 0.f, 0.f, 0.f, 0.f, 0.f, 0.f, 0.f, 0.f, 0.f}; return z; }
DI f32x4 zero4() { f32x4 z = {0.f, 0.f, 0.f, 0.f}; return z; }
#define PIN(x) asm volatile("" : "+v"(x))
#define DRAIN() asm volatile("s_waitcnt vmcnt(0)" ::: "memory")
#define BAR() do { asm volatile("s_waitcnt lgkmcnt(0)" ::: "memory"); __builtin_amdgcn_s_barrier(); asm volatile("" ::: "memory"); } while (0)
#define ST_AUX 16
#define LD_AUX 16
#define LDB_AUX 16
#define DLY_H 10
DI void st16(rsrc_t rs, unsigned off, unsigned so, u32x4 v) { __builtin_amdgcn_raw_buffer_store_b128(v, rs, off, so, ST_AUX); }
DI u32x4 ld16(rsrc_t rs, unsigned off, unsigned so) { return __builtin_amdgcn_raw_buffer_load_b128(rs, off, so, LD_AUX); }
DI u32x4 ld16b(rsrc_t rs, unsigned off, unsigned so) { return __builtin_amdgcn_raw_buffer_load_b128(rs, off, so, LDB_AUX); }
DI unsigned ld4(rsrc_t rs, unsigned off, unsigned so) { return __builtin_amdgcn_raw_buffer_load_b32(rs, off, so, 16); }
DI void st4(rsrc_t rs, unsigned off, unsigned so, unsigned v) { __builtin_amdgcn_raw_buffer_store_b32(v, rs, off, so, 16); }
DI unsigned xor3(unsigned a, unsigned b, unsigned c) { return __builtin_amdgcn_bitop3_b32(a, b, c, 0x96); }
DI u32x4 piece(unsigned a, unsigned b, unsigned c, unsigned tag) { u32x4 v = {a, b, c, xor3(a, b, c) ^ tag}; return v; }
DI f32x4 asf(u32x4 v) { return __builtin_bit_cast(f32x4, v); }
DI f32x4 sub4(const f32x16& x, int o) { f32x4 r = {x[o], x[o + 1], x[o + 2], x[o + 3]}; return r; }
DI u32x4 pk44(f32x4 a, f32x4 b) { u32x4 r = {pk(a[0], a[1]), pk(a[2], a[3]), pk(b[0], b[1]), pk(b[2], b[3])}; return r; }
DI u32x4 pkc(f32x4 re, f32x4 im) { u32x4 r = {pk(re[0], im[0]), pk(re[1], im[1]), pk(re[2], im[2]), pk(re[3], im[3])}; return r; }
DI rsrc_t mkrs(const void* p, unsigned bytes) { return __builtin_amdgcn_make_buffer_rsrc((void*)p, 0, bytes, 0x00020000); }
template <int NT>
DI void dma16(const void* g, unsigned lds) {
    unsigned keep;
    if (NT) asm volatile("s_mov_b32 %0, m0\n\ts_mov_b32 m0, %2\n\ts_nop 0\n\tglobal_load_lds_dwordx4 %1, off nt\n\ts_mov_b32 m0, %0" : "=&s"(keep) : "v"(g), "s"(lds) : "memory");
    else asm volatile("s_mov_b32 %0, m0\n\ts_mov_b32 m0, %2\n\ts_nop 0\n\tglobal_load_lds_dwordx4 %1, off\n\ts_mov_b32 m0, %0" : "=&s"(keep) : "v"(g), "s"(lds) : "memory");
}
#define OUTB_AUX 0

DI void fetch4(rsrc_t rs, unsigned off, unsigned stride, unsigned tag, u32x4 (&f)[3]) {
    u32x4 x[4];
#pragma unroll 1
    for (unsigned spins = 0; spins < SPIN_CAP; ++spins) {
        asm volatile("" ::: "memory");
        unsigned c[4];
#pragma unroll
        for (int t = 0; t < 4; ++t) x[t] = ld16(rs, off, t * stride);
#pragma unroll
        for (int t = 0; t < 4; ++t) c[t] = xor3(xor3(x[t][0], x[t][1], x[t][2]), x[t][3], tag);
        if (__builtin_amdgcn_ballot_w64((c[0] | c[1] | c[2] | c[3]) != 0) == 0) break;
    }
#pragma unroll
    for (int e = 0; e < 3; ++e) f[e] = u32x4{x[0][e], x[1][e], x[2][e], x[3][e]};
}

template <int CTRL, int BANK>
DI float dppo(float old, float v) {
    return __int_as_float(__builtin_amdgcn_update_dpp(__float_as_int(old), __float_as_int(v), CTRL, 0xf, BANK, false));
}
template <int CTRL>
DI float dppz(float v) { return __int_as_float(__builtin_amdgcn_update_dpp(0, __float_as_int(v), CTRL, 0xf, 0xf, true)); }
DI void reduce_tile(const f32x16& a, f32x4& o, float& c) {
    float r0 = a[0], r1 = a[1], r2 = a[2], r3 = a[3], r4 = a[4], r5 = a[5], r6 = a[6], r7 = a[7];
    float r8 = a[8], r9 = a[9], r10 = a[10], r11 = a[11], r12 = a[12], r13 = a[13], r14 = a[14], r15 = a[15];
    asm volatile(
        "s_nop 7\n\t"
        "s_nop 3\n\t"
        "v_mov_b32 %16, 0\n\t"
        "v_add_f32_dpp %0, %1, %0 row_shr:4 row_mask:0xf bank_mask:0xa\n\t"
        "v_add_f32_dpp %2, %3, %2 row_shr:4 row_mask:0xf bank_mask:0xa\n\t"
        "v_add_f32_dpp %4, %5, %4 row_shr:4 row_mask:0xf bank_mask:0xa\n\t"
        "v_add_f32_dpp %6, %7, %6 row_shr:4 row_mask:0xf bank_mask:0xa\n\t"
        "v_add_f32_dpp %8, %9, %8 row_shr:4 row_mask:0xf bank_mask:0xa\n\t"
        "v_add_f32_dpp %10, %11, %10 row_shr:4 row_mask:0xf bank_mask:0xa\n\t"
        "v_add_f32_dpp %12, %13, %12 row_shr:4 row_mask:0xf bank_mask:0xa\n\t"
        "v_add_f32_dpp %14, %15, %14 row_shr:4 row_mask:0xf bank_mask:0xa\n\t"
        "v_mov_b32_dpp %16, %15 row_shl:4 row_mask:0xf bank_mask:0x5\n\t"
        "v_add_f32_dpp %2, %1, %2 row_shl:4 row_mask:0xf bank_mask:0x5\n\t"
        "v_add_f32_dpp %6, %5, %6 row_shl:4 row_mask:0xf bank_mask:0x5\n\t"
        "v_add_f32_dpp %10, %9, %10 row_shl:4 row_mask:0xf bank_mask:0x5\n\t"
        "v_add_f32_dpp %14, %13, %14 row_shl:4 row_mask:0xf bank_mask:0x5\n\t"
        "v_add_f32_dpp %4, %3, %4 row_shl:4 row_mask:0xf bank_mask:0x5\n\t"
        "v_add_f32_dpp %8, %7, %8 row_shl:4 row_mask:0xf bank_mask:0x5\n\t"
        "v_add_f32_dpp %12, %11, %12 row_shl:4 row_mask:0xf bank_mask:0x5\n\t"
        "v_add_f32_dpp %0, %2, %0 row_ror:8 row_mask:0xf bank_mask:0xc\n\t"
        "v_add_f32_dpp %4, %6, %4 row_ror:8 row_mask:0xf bank_mask:0xc\n\t"
        "v_add_f32_dpp %8, %10, %8 row_ror:8 row_mask:0xf bank_mask:0xc\n\t"
        "v_add_f32_dpp %12, %14, %12 row_ror:8 row_mask:0xf bank_mask:0xc\n\t"
        "v_add_f32_dpp %16, %14, %16 row_ror:8 row_mask:0xf bank_mask:0x3\n\t"
        "v_add_f32_dpp %4, %2, %4 row_ror:8 row_mask:0xf bank_mask:0x3\n\t"
        "v_add_f32_dpp %8, %6, %8 row_ror:8 row_mask:0xf bank_mask:0x3\n\t"
        "v_add_f32_dpp %12, %10, %12 row_ror:8 row_mask:0xf bank_mask:0x3\n\t"
        : "+v"(r0), "+v"(r1), "+v"(r2), "+v"(r3), "+v"(r4), "+v"(r5), "+v"(r6), "+v"(r7), "+v"(r8), "+v"(r9), "+v"(r10), "+v"(r11),
          "+v"(r12), "+v"(r13), "+v"(r14), "+v"(r15), "=&v"(c));
    o = f32x4{r0, r4, r8, r12};
}

#define CODE_WGS 0
#define CODE_BYTES 21248
#define OUT_AUX 0

template <int V> struct IC { static constexpr int v = V; };
struct ConvCtx {
    int pa, pb, bz, pu, csrc, yo;
    float dv;
    bool first, diag;
};
DI float edge_sum(f32x4 d, bool diag) {
    float t = dppz<0x104>(d[3]) + dppz<0x108>(d[2]) + dppz<0x10c>(d[1]);
    t = diag ? t : 0.f;
    t = __builtin_amdgcn_mfma_f32_16x16x4f32(1.0f, t, zero4(), 0, 0, 0)[0];
    t += dppz<0xb1>(t);
    t += dppz<0x4e>(t);
    return t;
}
DI u32x2 finish_tile(const f32x16& acc, float edge, u32x2 u, const ConvCtx& c) {
    f32x4 o;
    float cy;
    reduce_tile(acc, o, cy);
    cy = __int_as_float(__builtin_amdgcn_ds_bpermute(c.csrc, __float_as_int(cy)));
    const f32x4 uf = {bfhi(u[1]), bflo(u[1]), bfhi(u[0]), bflo(u[0])};
    o = uf * c.dv + o;
    o[0] += c.first ? edge : cy;
    return u32x2{pk(o[0], o[1]), pk(o[2], o[3])};
}
template <int PART>
DI void conv(char* smem, const ConvCtx& c, const u32x4 (&ea)[4], u32x4 a0) {
    constexpr int T1 = 7 - PART, T2 = PART == 0 ? 3 : 2 * PART, T3 = 2 - PART;
    constexpr int NS = PART == 2 ? 24 : 16, PF = PART == 2 ? 2 : 1;
    constexpr int F2 = 4 * T2 + 4 + PF, F3 = PART == 0 ? -9 : (PART == 1 ? 8 + PF : NS + PF);
    const char* pa = smem + c.pa;
    const char* pb = smem + c.pb;
    u32x4 fa[32], fb[32], fc[8];
    f32x16 acc1 = zero16(), acc2 = zero16(), acc3 = zero16();
    u32x4 eb[7];
    u32x2 u1, u2, u3, y2;
    f32x4 d1 = zero4(), d2 = zero4(), d3 = zero4();
    float e1, e2, e3 = 0.f;
    fa[0] = a0;
#define LDA(m) fa[m] = trread2(pa + 128 * (m), pa + 128 * (m) + 32)
#define BFR(n) (*(const u32x4*)(pb + 128 * ((n) + 3)))
#define LDB(n) fb[(n) + 3] = BFR(n)
#define EB(T, i) (*(const u32x4*)(smem + c.bz + 512 * (T) - 1024 * (i)))
#pragma unroll
    for (int i = 0; 2 * i < T1; ++i) eb[i] = EB(T1, i);
#pragma unroll
    for (int i = 0; 2 * i < T2; ++i) eb[4 + i] = EB(T2, i);
    if (PART == 1) eb[6] = EB(1, 0);
#pragma unroll
    for (int s = 0; s < NS + PF; ++s) {
        asm volatile("" ::: "memory");
        if (s < NS) {
            if (s >= 1) LDA(s);
            if (PART == 0) { LDB(28 - s); LDB(12 - s); LDA(s + 16); }
            if (PART == 1) { LDB(24 - s); if (s < 12) { LDB(8 - s); LDA(s + 16); } }
            if (PART == 2) { if (s < 4) LDB(20 - s); if (s < 20) LDB(16 - s); }
            if (PART == 1 && s < 8) fc[s] = BFR(4 - s);
            if (PART == 2 && s >= 20) fc[s - 20] = trread2(pa + 128 * (s - 20), pa + 128 * (s - 20) + 32);
        }
        if (s == F3 - 1) u3 = *(const u32x2*)(smem + c.pu + 512 * T3);
        if (s == F2 - 1) u2 = *(const u32x2*)(smem + c.pu + 512 * T2);
        if (s == NS + PF - 1) u1 = *(const u32x2*)(smem + c.pu + 512 * T1);
        __builtin_amdgcn_sched_barrier(0);
        if (s == 0) {
#pragma unroll
            for (int i = 0; 2 * i < T1; ++i) {
                d1 = mfma16(ea[i], eb[i], d1);
                if (2 * i < T2) d2 = mfma16(ea[i], eb[4 + i], d2);
            }
            if (PART == 1) d3 = mfma16(ea[0], eb[6], d3);
        }
        if (s >= PF) {
            const int m = s - PF;
            acc1 = mfma(fa[m], fb[4 * T1 - m + 3], acc1);
            if (m < 4 * T2 + 4) acc2 = mfma(fa[m], fb[4 * T2 - m + 3], acc2);
            if (PART < 2 && m < 4 * T2 + 4) acc1 = mfma(fa[m + 16], fb[4 * T2 - m + 3], acc1);
            if (PART == 1 && m < 8) acc3 = mfma(fa[m], fc[m], acc3);
            if (PART == 2 && m >= 20) acc3 = mfma(fc[m - 20], fb[20 - m + 3], acc3);
        }
        if (s == PF) {
            e1 = edge_sum(d1, c.diag);
            e2 = edge_sum(d2, c.diag);
            if (PART == 1) e3 = edge_sum(d3, c.diag);
        }
        if (s == F3) *(u32x2*)(smem + c.yo + (LB_U + 3584 - LB_Y)) = finish_tile(acc3, e3, u3, c);
        if (s == F2) y2 = finish_tile(acc2, e2, u2, c);
        __builtin_amdgcn_sched_barrier(0);
    }
#undef LDA
#undef LDB
#undef BFR
#undef EB
    if (PART == 0) y2 = finish_tile(acc2, e2, u2, c);
    if (PART == 2) *(u32x2*)(smem + c.yo + (LB_U + 3584 - LB_Y)) = finish_tile(acc3, e3, u3, c);
    const u32x2 y1 = finish_tile(acc1, e1, u1, c);
    *(u32x2*)(smem + c.yo + 512) = y2;
    *(u32x2*)(smem + c.yo) = y1;
}

constexpr int LA_EX = 10240, LA_E1 = 28672, LA_T3 = 53248, LA_T1 = 67584;

__global__ __launch_bounds__(512, 4) void k_fused(const u16* __restrict__ x, const u16* __restrict__ wr, const u16* __restrict__ wi,
                                                  const u16* __restrict__ qq, const u16* __restrict__ kk, const u16* __restrict__ vv,
                                                  const u16* __restrict__ shk, const u16* __restrict__ hk, const u16* __restrict__ dd,
                                                  u16* __restrict__ out, char* ws) {
    __shared__ __attribute__((aligned(16))) char smem[LDS_BYTES];
    const int tid = threadIdx.x, lane = tid & 63;
    const int w = __builtin_amdgcn_readfirstlane(tid >> 6);
    const int n = lane & 31, hh = lane >> 5;
    const int g = lane >> 4, q = (lane >> 2) & 3, p = lane & 3;
    const unsigned lb = (unsigned)(size_t)(lds_vp)smem;
    STAMP_DECL
    STAMP(0);
    unsigned warm = 0;
    if (w >= 5 && blockIdx.x < CODE_WGS) {
        unsigned long long pc;
        asm volatile("s_getpc_b64 %0" : "=s"(pc));
        asm volatile("global_load_dword %0, %1, %2" : "=v"(warm) : "v"(min((tid - 320) * 128, CODE_BYTES)), "s"(pc) : "memory");
    }
    asm volatile("" :: "s"(x), "s"(wr), "s"(wi), "s"(qq), "s"(kk), "s"(vv), "s"(shk), "s"(hk), "s"(dd), "s"(out), "s"(ws));
    const rsrc_t rs = mkrs(ws, WS_END);
#define rs0 rs
    if (blockIdx.x < 256) {
        const int i2 = blockIdx.x;
        const bool hasU = i2 < 8 * 27;
        if (w < 4) {
            {
                const char* r0 = (const char*)(x + (size_t)(256 * (2 * w) + i2) * D_);
                dma16<NT_X>(r0 + lane * 16, lb + w * 2560);
                dma16<NT_X>(r0 + 256 * D_ * 2 + lane * 16, lb + w * 2560 + 1024);
                if (lane < 32) dma16<NT_X>(r0 + g * (256 * D_ * 2) + 1024 + (lane & 15) * 16, lb + w * 2560 + 2048);
            }
            unsigned ep = ld4(rs0, WS_EP, 0);
            u32x4 c1m[2];
#pragma unroll
            for (int t = 0; t < 4; ++t) {
                unsigned f = cs((float)((256 * (4 * hh + t) + i2) * (n >> 2)) * (1.0f / 2048.0f) + 0.25f * (n & 1));
                c1m[0][t] = (n & 2) ? 0u : f;
                c1m[1][t] = (n & 2) ? f : 0u;
            }
            const int pi = min(28 * w + n, PK - 1);
            int exr[3];
#pragma unroll
            for (int e = 0; e < 3; ++e) {
                int kd = min(3 * pi + e, 320);
                int m = (kd * 1639) >> 16, c = kd - 40 * m;
                int sa = 21 * m + c, sb = 355 - sa;
                exr[e] = LA_EX + (c <= 20 ? sa : sb) * 48 + 16 * hh;
                PIN(exr[e]);
            }
            PIN(c1m[0]); PIN(c1m[1]);
            int xao = w * 2560 + (g & 1) * 1024 + (8 * hh + q) * 32 + p * 8;
            int xto = w * 2560 + 2048 + (g & 1) * 256 + q * 32 + p * 8;
            int exw = LA_EX + (42 * hh + n) * 48 + 8 * w;
            int l16 = lane * 16 + 32768;
            unsigned rowoff = i2 * ROWB + pi * 16;
            unsigned x1o = (n < 28 && 28 * w + n < PK) ? WS_X1 + (i2 >> 1) * (2 * ROWB) + (i2 & 1) * 64 + (pi >> 2) * 128 + (pi & 3) * 16 + hh * (PK * 32) : 0x80000000u;
            PIN(xao); PIN(xto); PIN(exw); PIN(x1o); PIN(l16);
            STAMP(1);
            __builtin_amdgcn_s_setprio(2);
            PIN(ep);
            const unsigned tag = 2u * __builtin_amdgcn_readfirstlane(ep) + 3u;
            DRAIN();
            BAR();
            STAMP(2);
            {
                const char* xa = smem + xao;
                const char* xt = smem + xto;
                const char* tp = smem + l16 + (LA_T1 - 32768);
                u32x4 tb[12];
                u32x4 a0 = trread2(xa, xa + 128);
                tb[0] = *(const u32x4*)tp; tb[1] = *(const u32x4*)(tp + 1024);
                u32x4 a1 = trread2(xa + 512, xa + 640);
                tb[2] = *(const u32x4*)(tp + 2048); tb[3] = *(const u32x4*)(tp + 3072);
                u32x4 a2 = trread2(xt, xt + 128);
                tb[4] = *(const u32x4*)(tp + 4096); tb[5] = *(const u32x4*)(tp + 5120);
                __builtin_amdgcn_sched_barrier(0);
                f32x16 tr = mfma(a0, tb[0], zero16());
                f32x16 ti = mfma(a0, tb[1], zero16());
                __builtin_amdgcn_sched_barrier(0);
#pragma unroll
                for (int f = 11; f >= 6; --f) tb[f] = *(const u32x4*)(tp + f * 1024);
                __builtin_amdgcn_sched_barrier(0);
                tr = mfma(a1, tb[2], tr);
                ti = mfma(a1, tb[3], ti);
                tr = mfma(a2, tb[4], tr);
                ti = mfma(a2, tb[5], ti);
                f32x16 y[2];
#pragma unroll
                for (int ss = 0; ss < 2; ++ss) {
                    f32x4 r0 = sub4(tr, 8 * ss), r1 = sub4(tr, 8 * ss + 4), i0 = sub4(ti, 8 * ss), i1 = sub4(ti, 8 * ss + 4);
                    f32x4 m2 = r0 * asf(tb[10]), m3 = r1 * asf(tb[11]), m0 = i0 * asf(tb[10]), m1 = i1 * asf(tb[11]);
                    __builtin_amdgcn_sched_barrier(0);
                    m0 = r0 * asf(tb[8]) + m0; m1 = r1 * asf(tb[9]) + m1;
                    m2 = i0 * asf(tb[8]) - m2; m3 = i1 * asf(tb[9]) - m3;
                    __builtin_amdgcn_sched_barrier(0);
                    u32x4 pr = pk44(m0, m1), pi_ = pk44(m2, m3);
                    __builtin_amdgcn_sched_barrier(0);
                    y[ss] = mfma(tb[6], pr, zero16());
                    y[ss] = mfma(tb[7], pi_, y[ss]);
                }
                if (n <= 20) {
#pragma unroll
                    for (int t = 0; t < 8; ++t) {
                        u32x2 v = {pk(y[0][2 * t], y[0][2 * t + 1]), pk(y[1][2 * t], y[1][2 * t + 1])};
                        *(u32x2*)(smem + exw + 21 * ((t & 1) + 4 * (t >> 1)) * 48) = v;
                    }
                }
            }
            BAR();
            {
                const u32x4 b0 = *(const u32x4*)(smem + exr[0]), b2 = *(const u32x4*)(smem + exr[2]), b1 = *(const u32x4*)(smem + exr[1]);
                __builtin_amdgcn_sched_barrier(0);
                f32x16 o0 = mfma(c1m[0], b0, zero16());
                f32x16 o1 = mfma(c1m[0], b2, zero16());
                o0 = mfma(c1m[1], b1, o0);
                u32x4 pc[4];
#pragma unroll
                for (int gq = 0; gq < 4; ++gq)
                    pc[gq] = piece(pk(o0[4 * gq], o0[4 * gq + 1]), pk(o0[4 * gq + 2], o0[4 * gq + 3]), pk(o1[4 * gq], o1[4 * gq + 1]), tag);
#pragma unroll
                for (int gq = 0; gq < 4; ++gq) st16(rs, x1o, gq * 2 * PK * 32, pc[gq]);
            }
            STAMP(3);
            DRAIN();
            STAMP(4);
            BAR();
            u32x4 tc[14];
#pragma unroll
            for (int f = 0; f < 14; ++f) tc[f] = *(const u32x4*)(smem + l16 + (LA_T3 - 32768) + f * 1024);
            u32x4 ci[2];
#pragma unroll
            for (int t = 0; t < 4; ++t) {
                unsigned f = cs(0.25f * (n & 1) - (float)((256 * (n >> 2) + i2) * (4 * hh + t)) * (1.0f / 2048.0f));
                ci[0][t] = (n & 2) ? 0u : f;
                ci[1][t] = (n & 2) ? f : 0u;
            }
            PIN(ci[0]); PIN(ci[1]);
#pragma unroll
            for (int f = 0; f < 14; ++f) PIN(tc[f]);
            unsigned zoff = WS_Z1 + rowoff + 4 * hh * (PK * 16);
            const int k1c = min(n, 20);
            int e3o = (hh * 324 + 3 * pi) * 4, s0o = (2 * w * 324 + 160 * hh + k1c) * 4, s1o = (2 * w * 324 + 200 - 160 * hh - k1c) * 4;
            unsigned oo = ((256 * 2 * w + i2) * (2 * D_) + 16 * n + 8 * hh) * 2;
            unsigned oo2 = n < 8 ? oo + 1024 : 0x80000000u;
            PIN(zoff); PIN(e3o); PIN(s0o); PIN(s1o); PIN(oo); PIN(oo2);
            const rsrc_t ro = mkrs(out, S_ * 2 * D_ * 2);
            if (hasU) {
                BAR(); BAR(); BAR();
            } else {
#pragma unroll 1
                for (unsigned spins = 0; spins < SPIN_CAP; ++spins) {
                    __builtin_amdgcn_s_sleep(4);
                    asm volatile("" ::: "memory");
                    u32x4 v = ld16(rs, WS_Z1 + i2 * ROWB + w * 2 * PK * 16, 0);
                    if (__builtin_amdgcn_readfirstlane(v[0] ^ v[1] ^ v[2] ^ v[3]) == tag) break;
                }
            }
            STAMP(9);
            {
                u32x4 zb[3];
                fetch4(rs, zoff, PK * 16, tag, zb);
                STAMP(10);
                f32x16 o0 = mfma(ci[0], zb[0], zero16());
                f32x16 o1 = mfma(ci[0], zb[2], zero16());
                o0 = mfma(ci[1], zb[1], o0);
                unsigned* e3 = (unsigned*)(smem + e3o);
#pragma unroll
                for (int gq = 0; gq < 4; ++gq) {
                    e3[gq * 648] = pk(o0[4 * gq], o0[4 * gq + 1]);
                    e3[gq * 648 + 1] = pk(o0[4 * gq + 2], o0[4 * gq + 3]);
                    e3[gq * 648 + 2] = pk(o1[4 * gq], o1[4 * gq + 1]);
                }
            }
            BAR();
            if (i2 == 0 && tid == 0) st4(rs0, WS_EP, 0, ep + 1u);
            {
                const unsigned* s0 = (const unsigned*)(smem + s0o);
                const unsigned* s1 = (const unsigned*)(smem + s1o);
                const u32x4 f0 = {s0[0], s0[40], s0[80], s0[120]}, f1 = {s0[324], s0[364], s0[404], s0[444]};
                const u32x4 f2 = {s1[120], s1[80], s1[40], s1[0]}, f3 = {s1[444], s1[404], s1[364], s1[324]};
                __builtin_amdgcn_sched_barrier(0);
                f32x16 tr = mfma(f0, tc[0], zero16());
                f32x16 ti = mfma(f1, tc[0], zero16());
                tr = mfma(f2, tc[1], tr);
                ti = mfma(f3, tc[1], ti);
                f32x4 re[3], im[3], mr[3], mi[3];
#pragma unroll
                for (int r = 0; r < 12; ++r) {
                    auto sw = __builtin_amdgcn_permlane16_swap(__float_as_uint(tr[r]), __float_as_uint(ti[r]), false, false);
                    re[r >> 2][r & 3] = __uint_as_float(sw[0]);
                    im[r >> 2][r & 3] = __uint_as_float(sw[1]);
                }
                __builtin_amdgcn_sched_barrier(0);
#pragma unroll
                for (int j = 0; j < 3; ++j) { mr[j] = im[j] * asf(tc[11 + j]); mi[j] = re[j] * asf(tc[11 + j]); }
                __builtin_amdgcn_sched_barrier(0);
#pragma unroll
                for (int j = 0; j < 3; ++j) { mr[j] = re[j] * asf(tc[8 + j]) - mr[j]; mi[j] = im[j] * asf(tc[8 + j]) + mi[j]; }
                __builtin_amdgcn_sched_barrier(0);
                const u32x4 a0 = pk44(mr[0], mr[1]), a1 = pk44(mi[0], mi[1]), a2 = pk44(mr[2], mi[2]);
                f32x16 y0 = mfma(a0, tc[2], zero16());
                y0 = mfma(a1, tc[3], y0);
                y0 = mfma(a2, tc[4], y0);
                f32x16 y1 = mfma(a0, tc[5], zero16());
                y1 = mfma(a1, tc[6], y1);
                y1 = mfma(a2, tc[7], y1);
                __builtin_amdgcn_raw_buffer_store_b128(pack8(y0, 0), ro, oo, 0, OUT_AUX);
                __builtin_amdgcn_raw_buffer_store_b128(pack8(y0, 8), ro, oo, 256 * 2 * D_ * 2, OUT_AUX);
                __builtin_amdgcn_raw_buffer_store_b128(pack8(y1, 0), ro, oo2, 0, OUT_AUX);
                __builtin_amdgcn_raw_buffer_store_b128(pack8(y1, 8), ro, oo2, 256 * 2 * D_ * 2, OUT_AUX);
            }
            STAMP(11);
        } else {
            const int w2 = w - 4;
#pragma unroll
            for (int k = 0; k < 3; ++k) dma16<0>((const char*)g_t1.v + (3 * w2 + k) * 1024 + lane * 16, lb + LA_T1 + (3 * w2 + k) * 1024);
            const unsigned ep = ld4(rs0, WS_EP, 0);
            const int k1s = i2 & 7, pi = 4 * (i2 >> 3) + p;
            const unsigned we = (4 * w2 + q + 64 * g) * NW + k1s * DF + 3 * pi;
            unsigned vo = hasU ? (we * 2) & ~3u : 0x80000000u;
            PIN(vo);
            u32x4 fre, fim, fin;
            f32x4 tc, ts;
#pragma unroll
            for (int t = 0; t < 4; ++t) {
                float an = (float)((lane & 15) * (4 * g + t)) * (1.0f / 16.0f);
                float c = fcos(an), s = fsin(an);
                fre[t] = pk(c, s);
                fim[t] = pk(-s, c);
                fin[t] = fim[t] ^ 0x80008000u;
                float b = (float)((4 * w2 + q) * (4 * g + t)) * (1.0f / 256.0f);
                tc[t] = fcos(b); ts[t] = fsin(b);
            }
            PIN(fre); PIN(fim); PIN(fin); PIN(tc); PIN(ts);
            DRAIN();
            BAR();
            asm volatile("" :: "v"(warm));
            u32x2 wraw[4][2];
            {
                const rsrc_t rwr = mkrs(wr, S_ * DF * 2), rwi = mkrs(wi, S_ * DF * 2);
#pragma unroll
                for (int r = 0; r < 4; ++r) {
                    wraw[r][0] = __builtin_amdgcn_raw_buffer_load_b64(rwr, vo, r * 16 * NW * 2, NT_W);
                    wraw[r][1] = __builtin_amdgcn_raw_buffer_load_b64(rwi, vo, r * 16 * NW * 2, NT_W);
                }
            }
#pragma unroll
            for (int k = 0; k < 4; ++k) {
                const int f = min(4 * w2 + k, 13);
                dma16<0>((const char*)g_t3.v + f * 1024 + lane * 16, lb + LA_T3 + f * 1024);
            }
            BAR();
            DRAIN();
            f32x4 wf[2][3];
            {
                const unsigned sh = (we & 1) * 16;
#pragma unroll
                for (int r = 0; r < 4; ++r)
#pragma unroll
                    for (int a = 0; a < 2; ++a) {
                        unsigned lo = __builtin_amdgcn_alignbit(wraw[r][a][1], wraw[r][a][0], sh), hi = wraw[r][a][1] >> sh;
                        wf[a][0][r] = bflo(lo); wf[a][1][r] = bfhi(lo); wf[a][2][r] = bflo(hi);
                    }
#pragma unroll
                for (int e = 0; e < 3; ++e) { PIN(wf[0][e]); PIN(wf[1][e]); }
            }
            const unsigned tag = 2u * __builtin_amdgcn_readfirstlane(ep) + 3u;
            unsigned coloff = (64 * g + 4 * w2 + q) * ROWB + (k1s * PK + pi) * 16;
            int ewo = LA_E1 + (g * 768 + (16 * w2 + p) * 4 + q) * 4, ero = LA_E1 + w2 * 3072 + lane * 16;
            unsigned xoff = WS_X1 + (64 * g + 4 * w2 + (q & 2)) * ROWB + (k1s * 27 + (i2 >> 3)) * 128 + (lane & 7) * 16;
            PIN(coloff); PIN(ewo); PIN(ero); PIN(xoff);
            unsigned* ew = (unsigned*)(smem + ewo);
            const char* er = smem + ero;
            STAMP(1);
            __builtin_amdgcn_s_setprio(2);
            BAR();
            if (hasU) {
            STAMP(5);
            u32x4 f[3];
            f32x4 re[3], im[3], mr[3], mi[3];
            fetch4(rs, xoff, 16 * ROWB, tag, f);
            STAMP(6);
#define DFT16(FI) _Pragma("unroll") for (int e = 0; e < 3; ++e) { re[e] = mfma16(fre, f[e], zero4()); im[e] = mfma16(FI, f[e], zero4()); }
#define TWID_STORE(OFF) \
            _Pragma("unroll") for (int e = 0; e < 3; ++e) { \
                mr[e] = im[e] * ts; mi[e] = re[e] * ts; \
                mr[e] = re[e] * tc + mr[e]; mi[e] = im[e] * tc - mi[e]; \
                u32x4 v = pkc(mr[e], mi[e]); \
                _Pragma("unroll") for (int r = 0; r < 4; ++r) ew[OFF + e * 256 + r * 16] = v[r]; \
            }
            DFT16(fim);
            __builtin_amdgcn_sched_barrier(0);
            TWID_STORE(0);
            BAR();
#pragma unroll
            for (int e = 0; e < 3; ++e) f[e] = *(const u32x4*)(er + e * 1024);
            __builtin_amdgcn_sched_barrier(0);
            DFT16(fin);
            __builtin_amdgcn_sched_barrier(0);
#pragma unroll
            for (int e = 0; e < 3; ++e) {
                mr[e] = im[e] * wf[1][e]; mi[e] = re[e] * wf[1][e];
                mr[e] = re[e] * wf[0][e] + mr[e]; mi[e] = im[e] * wf[0][e] - mi[e];
                f[e] = pkc(mr[e], mi[e]);
                re[e] = mfma16(fre, f[e], zero4()); im[e] = mfma16(fim, f[e], zero4());
            }
            __builtin_amdgcn_sched_barrier(0);
            TWID_STORE(3072);
            BAR();
#pragma unroll
            for (int e = 0; e < 3; ++e) f[e] = *(const u32x4*)(er + 12288 + e * 1024);
            __builtin_amdgcn_sched_barrier(0);
            DFT16(fin);
            __builtin_amdgcn_sched_barrier(0);
#pragma unroll
            for (int e = 0; e < 3; ++e) f[e] = pkc(re[e], im[e]);
#pragma unroll
            for (int r = 0; r < 4; ++r) st16(rs, coloff, WS_Z1 + r * 16 * ROWB, piece(f[0][r], f[1][r], f[2][r], tag));
            STAMP(7);
            DRAIN();
            STAMP(8);
            BAR();
            }
        }
    } else {
        const int bb = blockIdx.x - 256;
        const int xg = bb & 7, ib = bb >> 3;
        const bool cons = ib < 30;
        const int gl = min(ib / 3, 9), part = ib - 3 * (ib / 3);
        const int ch0 = 80 * xg + 8 * gl;
        const unsigned epo = WS_EP + 256 * (1 + xg);
        unsigned ep;
        if (w < 5) {
            __builtin_amdgcn_s_setprio(1);
            const int rg = tid / 40, cp = tid - 40 * rg;
            const int chp = 80 * xg + 2 * cp, s0 = 64 * ib + 8 * rg;
            const rsrc_t rk = mkrs(kk, S_ * D_ * 2), rv = mkrs(vv, S_ * D_ * 2);
            const unsigned vo = (s0 * D_ + chp) * 2;
            const unsigned ho = s0 > 0 ? vo - 3 * D_ * 2 : 0x80000000u;
            unsigned kr[11], vr[8];
            const u32x4 sw = *(const u32x4*)(shk + chp * 4);
#pragma unroll
            for (int i = 0; i < 3; ++i) kr[i] = __builtin_amdgcn_raw_buffer_load_b32(rk, ho + i * D_ * 2, 0, NT_KV);
#pragma unroll
            for (int i = 0; i < 8; ++i) {
                kr[3 + i] = __builtin_amdgcn_raw_buffer_load_b32(rk, vo + (i % 3) * D_ * 2, (i / 3) * 3 * D_ * 2, NT_KV);
                vr[i] = __builtin_amdgcn_raw_buffer_load_b32(rv, vo + (i % 3) * D_ * 2, (i / 3) * 3 * D_ * 2, NT_KV);
            }
            ep = ld4(rs, epo, 0);
            __builtin_amdgcn_sched_barrier(0);
            STAMP(1);
            f32x2 wt[4] = {{bflo(sw[0]), bflo(sw[2])}, {bfhi(sw[0]), bfhi(sw[2])}, {bflo(sw[1]), bflo(sw[3])}, {bfhi(sw[1]), bfhi(sw[3])}};
            f32x2 kf[11], o[8];
#pragma unroll
            for (int i = 0; i < 11; ++i) {
                kf[i] = f32x2{bflo(kr[i]), bfhi(kr[i])};
            }
#pragma unroll
            for (int e = 0; e < 8; ++e) {
                f32x2 a = kf[e] * wt[3];
                a = kf[e + 1] * wt[2] + a;
                a = kf[e + 2] * wt[1] + a;
                a = kf[e + 3] * wt[0] + a;
                o[e] = a * f32x2{bflo(vr[e]), bfhi(vr[e])};
            }
#pragma unroll
            for (int c = 0; c < 2; ++c) {
                char* dst = smem + LB_ST + (c * 40 + cp) * 136 + rg * 16;
                *(u32x2*)dst = u32x2{pk(o[0][c], o[1][c]), pk(o[2][c], o[3][c])};
                *(u32x2*)(dst + 8) = u32x2{pk(o[4][c], o[5][c]), pk(o[6][c], o[7][c])};
            }
            PIN(ep);
            STAMP(2);
            BAR();
            const char* src = smem + LB_ST + ((p >> 1) * 40 + 8 * w + g) * 136 + 64 * (p & 1) + 8 * (7 - q);
            const unsigned mo = WS_KV + ((xg * 10 + 2 * w) * 32 + ib) * MSGB;
            const u32x4 u0 = trread2(src, src - 32), u1 = trread2(src + 4 * 136, src + 4 * 136 - 32);
            st16(rs, lane * 16, mo, u0);
            st16(rs, lane * 16, mo + 32 * MSGB, u1);
            STAMP(3);
            DRAIN();
            if (lane < 2) st4(rs, lane * (32 * MSGB) + 1024, mo, 2u * ep + 3u);
            __builtin_amdgcn_s_setprio(0);
            STAMP(4);
        } else {
            ep = ld4(rs, epo, 0);
            const int t3 = tid - 320;
#pragma unroll
            for (int k = 0; k < 2; ++k) {
                const int z = t3 + 192 * k, c = z / 36, o = z - 36 * c;
                if (z < 288) *(u32x4*)(smem + c * CHB + (o < 4 ? o * 16 : LB_Z + (o - 4) * 16)) = u32x4{0u, 0u, 0u, 0u};
            }
            PIN(ep);
            if (cons) {
                __builtin_amdgcn_s_sleep(DLY_H);
                const char* hp = (const char*)(hk + (size_t)ch0 * S_) + lane * 16;
#pragma unroll 1
                for (int k = w - 5; k < 32; k += 3) dma16<NT_H>(hp + k * 1024, lb + (k >> 2) * CHB + LB_H + (k & 3) * 1024);
            }
            BAR();
            DRAIN();
        }
        if (cons) {
            const unsigned tag = 2u * __builtin_amdgcn_readfirstlane(ep) + 3u;
            ConvCtx c;
            {
                const int pi = 2 * (g & 1) + (p >> 1) + 4 * (p & 1);
                const int a = (n & 3) | ((n >> 4) << 2), am = (a + 7) & 7;
                c.pa = w * CHB + 8 + 8 * (8 * hh + q + pi);
                c.pb = w * CHB + LB_U + 64 * (a - hh) + 16 * q - 384;
                c.bz = w * CHB + LB_U + 16 * q - 64 * (1 + 4 * p + g);
                c.pu = w * CHB + LB_U + 64 * a + 16 * q + 8 - 8 * hh;
                c.csrc = 4 * ((hh ? n : ((am & 3) | (q << 2) | ((am >> 2) << 4))) + 32 - 32 * hh);
                c.yo = w * CHB + LB_Y + lane * 8;
                c.dv = bflo(dd[ch0 + w]);
                c.first = (lane & 0x33) == 0;
                c.diag = g == p;
                PIN(c.pa); PIN(c.pb); PIN(c.bz); PIN(c.pu); PIN(c.csrc); PIN(c.yo); PIN(c.dv);
            }
            const int nk = (32 - 4 * part - w + 7) >> 3;
            const unsigned mo = WS_KV + ((xg * 10 + gl) * 32 + w) * MSGB;
            const unsigned m3 = (nk - 1) * 8;
            unsigned rowa, rowb;
            {
                const int T1 = 7 - part, T2 = part == 0 ? 3 : 2 * part, T3 = 2 - part;
                const int lq = (tid >> 2) & 63, nq = lq & 31;
                const int row3 = 32 * ((nq & 3) | ((nq >> 4) << 2)) + 16 * (lq >> 5) + ((nq >> 2) & 3) + 4 * p;
                rowa = (row3 + 256 * (w < 4 ? T1 : T2)) * (D_ * 2);
                rowb = (row3 + ((part > 0 && w < 4) ? 256 * T3 : 0x100000)) * (D_ * 2);
            }
            int uo = (lane >> 3) * CHB + LB_U + w * 128 + (lane & 7) * 16;
            int yso = q * CHB + LB_Y + (w * 64 + g * 16 + p * 4) * 2;
            unsigned fo = min(p, nk - 1) * 8 * MSGB + 1024;
            int bao = w * CHB + LB_H + 8 * (32 * p + 8 * g + q);
            unsigned qoa = rowa, qob = rowb;
            PIN(rowa); PIN(rowb); PIN(uo); PIN(yso); PIN(fo); PIN(bao);
            BAR();
            auto tail = [&](auto part_c) __attribute__((always_inline)) {
            constexpr int PART = decltype(part_c)::v;
            u32x4 ea[4];
#pragma unroll
            for (int i = 0; i < 4; ++i) { ea[i] = trread2(smem + bao + 1024 * i, smem + bao + 1024 * i + 32); PIN(ea[i]); }
            u32x4 a0 = trread2(smem + c.pa, smem + c.pa + 32);
            PIN(a0);
#pragma unroll 1
            for (unsigned spins = 0; spins < SPIN_CAP; ++spins) {
                asm volatile("" ::: "memory");
                unsigned fl = ld4(rs, fo, mo);
                COUNT(13);
                if (__builtin_amdgcn_ballot_w64(fl != tag) == 0) break;
                __builtin_amdgcn_s_sleep(2);
            }
            asm volatile("" ::: "memory");
            STAMP(5);
            {
                u32x4 um[4];
#pragma unroll
                for (int k = 0; k < 4; ++k) um[k] = ld16b(rs, lane * 16, mo + (k < 3 ? 8 * k : m3) * MSGB);
#pragma unroll
                for (int k = 0; k < 3; ++k) *(u32x4*)(smem + uo + 1024 * k) = um[k];
                *(u32x4*)(smem + uo + 128 * m3) = um[3];
            }
            STAMP(6);
            BAR();
            STAMP(9);
            const rsrc_t rq = mkrs(qq, S_ * D_ * 2), ro = mkrs(out, S_ * 2 * D_ * 2);
            const u32x4 qa = __builtin_amdgcn_raw_buffer_load_b128(rq, qoa, ch0 * 2, NT_Q);
            const u32x4 qb = __builtin_amdgcn_raw_buffer_load_b128(rq, qob, ch0 * 2, NT_Q);
            __builtin_amdgcn_sched_barrier(0);
            conv<PART>(smem, c, ea, a0);
            STAMP(7);
            BAR();
            {
                const char* ys = smem + yso;
                const u32x4 y = trread2(ys, ys + 4 * CHB), z = trread2(ys + (LB_U + 3584 - LB_Y), ys + (LB_U + 3584 - LB_Y) + 4 * CHB);
                u32x4 o;
#pragma unroll
                for (int k = 0; k < 4; ++k) o[k] = pk(bflo(y[k]) * bflo(qa[k]), bfhi(y[k]) * bfhi(qa[k]));
                __builtin_amdgcn_raw_buffer_store_b128(o, ro, 2 * rowa, (D_ + ch0) * 2, OUTB_AUX);
#pragma unroll
                for (int k = 0; k < 4; ++k) o[k] = pk(bflo(z[k]) * bflo(qb[k]), bfhi(z[k]) * bfhi(qb[k]));
                __builtin_amdgcn_raw_buffer_store_b128(o, ro, 2 * rowb, (D_ + ch0) * 2, OUTB_AUX);
            }
            };
            if (part == 0) tail(IC<0>{});
            else if (part == 1) tail(IC<1>{});
            else tail(IC<2>{});
            if (ib == 0 && tid == 0) st4(rs, epo, 0, ep + 1u);
            STAMP(8);
        }
    }
    STAMP_OUT;
}
extern "C" void kernel_launch(void* const* d_in, const int* in_sizes, int n_in, void* d_out, int out_size, void* d_ws,
                              size_t ws_size, hipStream_t stream) {
    k_fused<<<512, 512, 0, stream>>>((const u16*)d_in[0], (const u16*)d_in[1], (const u16*)d_in[2], (const u16*)d_in[3],
                                     (const u16*)d_in[4], (const u16*)d_in[5], (const u16*)d_in[6], (const u16*)d_in[7],
                                     (const u16*)d_in[8], (u16*)d_out, (char*)d_ws + WSOFF);
}
```

```cpp
#define DI_ROLE 2
#define DI_FROM 0
#define DI_IDX 15
#define WSOFF ((ws_size - (16u << 20)) & ~(size_t)0x1fffff)
#define NT_X 0
#define NT_KV 0
#define NT_W 0
#define NT_H 0
#define NT_Q 0
#include <hip/hip_runtime.h>
#include <hip/hip_bf16.h>

typedef __attribute__((ext_vector_type(4))) short s16x4;
typedef __attribute__((ext_vector_type(2))) __bf16 bf16x2;
typedef __attribute__((ext_vector_type(8))) __bf16 bf16x8;
typedef __attribute__((ext_vector_type(2))) float f32x2;
typedef __attribute__((ext_vector_type(4))) float f32x4;
typedef __attribute__((ext_vector_type(16))) float f32x16;
typedef __attribute__((ext_vector_type(2))) unsigned u32x2;
typedef __attribute__((ext_vector_type(4))) unsigned u32x4;
typedef __attribute__((address_space(3))) s16x4* lds_s16x4p;
typedef __attribute__((address_space(3))) void* lds_vp;
typedef unsigned short u16;
typedef __amdgpu_buffer_rsrc_t rsrc_t;

#define DI __device__ __forceinline__

#define STAMP(I) do { if ((I) == 0 || (I) == DI_IDX || (I) == DI_FROM) { __builtin_amdgcn_sched_barrier(0); asm volatile("s_memrealtime %0\n\ts_waitcnt lgkmcnt(0)" : "=s"(st_[(I) == 0 ? 0 : ((I) == DI_IDX ? 1 : 2)]) :: "memory"); __builtin_amdgcn_sched_barrier(0); } } while (0)
#define STAMP_DECL unsigned long long st_[3] = {0, 0, 0};
#define STAMP_OUT do { if (DI_IDX == 14) { asm volatile("s_waitcnt vmcnt(0)" ::: "memory"); STAMP(14); } const bool sel_ = blockIdx.x == 256 && w == 0; \
    unsigned long long d_ = (sel_ && st_[1]) ? st_[1] - st_[DI_FROM ? 2 : 0] : 0; while (sel_ && __builtin_amdgcn_s_memrealtime() - st_[0] < 2500 + 2 * d_) __builtin_amdgcn_s_sleep(2); } while (0)
#define COUNT(I)

namespace tab {
constexpr double PI = 3.14159265358979323846;
constexpr double csin(double x) { double t = x, s = x; for (int i = 1; i < 12; ++i) { t *= -x * x / ((2 * i) * (2 * i + 1)); s += t; } return s; }
constexpr double ccos(double x) { double t = 1, s = 1; for (int i = 1; i < 12; ++i) { t *= -x * x / ((2 * i - 1) * (2 * i)); s += t; } return s; }
struct Base { float c[640]; };
constexpr Base mkbase() {
    Base b{};
    for (int i = 0; i < 640; ++i) {
        int m = i > 320 ? 640 - i : i;
        b.c[i] = m <= 160 ? (m <= 80 ? (float)ccos(2 * PI * m / 640) : (float)csin(2 * PI * (160 - m) / 640))
                          : (m >= 240 ? (float)-ccos(2 * PI * (320 - m) / 640) : (float)-csin(2 * PI * (m - 160) / 640));
    }
    return b;
}
constexpr Base base = mkbase();
constexpr float C(int n) { return base.c[((n % 640) + 640) % 640]; }
constexpr float S(int n) { return C(n - 160); }
constexpr unsigned fb(float f) { return __builtin_bit_cast(unsigned, f); }
constexpr unsigned bf(float f) { unsigned u = fb(f); return (u + 0x7fffu + ((u >> 16) & 1u)) >> 16; }
constexpr unsigned p2(float lo, float hi) { return bf(lo) | (bf(hi) << 16); }
struct T1 { unsigned v[12 * 256]; };
struct T3 { unsigned v[14 * 256]; };
constexpr T1 mk1() {
    T1 t{};
    for (int l = 0; l < 64; ++l) {
        const int n = l & 31, hh = l >> 5;
        for (int k = 0; k < 4; ++k) {
            for (int sg = 0; sg < 3; ++sg) {
                const int d1 = 16 * sg + 8 * hh + 2 * k;
                const bool ok = d1 < 40;
                t.v[((2 * sg) * 64 + l) * 4 + k] = ok ? p2(C(16 * d1 * n), C(16 * (d1 + 1) * n)) : 0u;
                t.v[((2 * sg + 1) * 64 + l) * 4 + k] = ok ? p2(-S(16 * d1 * n), -S(16 * (d1 + 1) * n)) : 0u;
            }
            const int k2 = n >> 1, ph = (n & 1) ? (k2 >= 8 ? -160 : 160) : 0;
            const int d2 = 2 * (k & 1) + 8 * (k >> 1) + 4 * hh;
            t.v[(6 * 64 + l) * 4 + k] = p2(C(40 * d2 * k2 + ph), C(40 * (d2 + 1) * k2 + ph));
            t.v[(7 * 64 + l) * 4 + k] = p2(S(40 * d2 * k2 + ph), S(40 * (d2 + 1) * k2 + ph));
            for (int e = 0; e < 2; ++e) {
                t.v[((8 + e) * 64 + l) * 4 + k] = fb(C((k + 8 * e + 4 * hh) * n));
                t.v[((10 + e) * 64 + l) * 4 + k] = fb(S((k + 8 * e + 4 * hh) * n));
            }
        }
    }
    return t;
}
constexpr float wt(int k1) { return k1 > 20 ? 0.f : ((k1 == 0 || k1 == 20) ? 1.0f : 2.0f) * (1.0f / (2048.0f * 640.0f)); }
constexpr T3 mk3() {
    T3 t{};
    for (int l = 0; l < 64; ++l) {
        const int n = l & 31, hh = l >> 5, e = n & 15, ro = n >> 4;
        const int d2 = (e & 3) + 8 * ((e >> 2) & 1) + 4 * (e >> 3);
        for (int k = 0; k < 4; ++k) {
            const int a0 = 160 * ro - 40 * d2 * (4 * hh + k), a1 = 40 * d2 * (8 + 4 * hh + k) - 160 * ro;
            t.v[(0 * 64 + l) * 4 + k] = p2(C(a0), S(a0));
            t.v[(1 * 64 + l) * 4 + k] = p2(C(a1), S(a1));
            for (int tl = 0; tl < 2; ++tl) {
                const int d1 = 32 * tl + n;
                const int ka = 2 * (k & 1) + 8 * (k >> 1) + 4 * hh, kb = 16 + 2 * (k & 1) + 4 * hh;
                t.v[((2 + 3 * tl) * 64 + l) * 4 + k] = p2(wt(ka) * C(16 * d1 * ka), wt(ka + 1) * C(16 * d1 * (ka + 1)));
                t.v[((3 + 3 * tl) * 64 + l) * 4 + k] = p2(-wt(ka) * S(16 * d1 * ka), -wt(ka + 1) * S(16 * d1 * (ka + 1)));
                t.v[((4 + 3 * tl) * 64 + l) * 4 + k] = k < 2 ? p2(wt(kb) * C(16 * d1 * kb), wt(kb + 1) * C(16 * d1 * (kb + 1)))
                                                             : p2(-wt(kb) * S(16 * d1 * kb), -wt(kb + 1) * S(16 * d1 * (kb + 1)));
            }
            for (int j = 0; j < 3; ++j) {
                t.v[((8 + j) * 64 + l) * 4 + k] = fb(C(d2 * (k + 8 * j + 4 * hh)));
                t.v[((11 + j) * 64 + l) * 4 + k] = fb(S(d2 * (k + 8 * j + 4 * hh)));
            }
        }
    }
    return t;
}
}
__device__ __attribute__((aligned(128))) const tab::T1 g_t1 = tab::mk1();
__device__ __attribute__((aligned(128))) const tab::T3 g_t3 = tab::mk3();

constexpr int S_ = 2048, D_ = 640, DF = 321;
constexpr int NW = 8 * DF;
constexpr int PK = 108;
constexpr int ROWB = 8 * PK * 16;
constexpr int MSGB = 1088;
constexpr unsigned WS_X1 = 0, WS_Z1 = 4u << 20, WS_KV = 8u << 20, WS_EP = 12u << 20, WS_END = 13u << 20;
constexpr unsigned SPIN_CAP = 1u << 17;
constexpr int CHB = 8832, LB_H = 64, LB_Y = 2112, LB_Z = 4160, LB_U = 4672, LB_ST = 8 * CHB, LDS_BYTES = LB_ST + 80 * 136;

DI unsigned pk(float lo, float hi) {
    f32x2 v = {lo, hi};
    return __builtin_bit_cast(unsigned, __builtin_convertvector(v, bf16x2));
}
DI float bflo(unsigned u) { return __uint_as_float(u << 16); }
DI float bfhi(unsigned u) { return __uint_as_float(u & 0xffff0000u); }
DI f32x16 mfma(u32x4 a, u32x4 b, f32x16 c) {
    return __builtin_amdgcn_mfma_f32_32x32x16_bf16(__builtin_bit_cast(bf16x8, a), __builtin_bit_cast(bf16x8, b), c, 0, 0, 0);
}
DI f32x4 mfma16(u32x4 a, u32x4 b, f32x4 c) {
    return __builtin_amdgcn_mfma_f32_16x16x32_bf16(__builtin_bit_cast(bf16x8, a), __builtin_bit_cast(bf16x8, b), c, 0, 0, 0);
}
DI float fsin(float rev) { return __builtin_amdgcn_sinf(rev); }
DI float fcos(float rev) { return __builtin_amdgcn_cosf(rev); }
DI unsigned cs(float rev) { return pk(fcos(rev), fsin(rev)); }
DI u32x4 trread2(const char* p0, const char* p1) {
    s16x4 a = __builtin_amdgcn_ds_read_tr16_b64_v4i16((lds_s16x4p)p0);
    s16x4 b = __builtin_amdgcn_ds_read_tr16_b64_v4i16((lds_s16x4p)p1);
    return __builtin_bit_cast(u32x4, __builtin_shufflevector(a, b, 0, 1, 2, 3, 4, 5, 6, 7));
}
DI u32x4 pack8(const f32x16& x, int o) {
    u32x4 r = {pk(x[o], x[o + 1]), pk(x[o + 2], x[o + 3]), pk(x[o + 4], x[o + 5]), pk(x[o + 6], x[o + 7])};
    return r;
}
DI f32x16 zero16() { f32x16 z = {0.f, 0.f, 0.f, 0.f, 0.f, 0.f, 0.f, 0.f, 0.f, 0.f, 0.f, 0.f, 0.f, 0.f, 0.f, 0.f}; return z; }
DI f32x4 zero4() { f32x4 z = {0.f, 0.f, 0.f, 0.f}; return z; }
#define PIN(x) asm volatile("" : "+v"(x))
#define DRAIN() asm volatile("s_waitcnt vmcnt(0)" ::: "memory")
#define BAR() do { asm volatile("s_waitcnt lgkmcnt(0)" ::: "memory"); __builtin_amdgcn_s_barrier(); asm volatile("" ::: "memory"); } while (0)
#define ST_AUX 16
#define LD_AUX 16
#define LDB_AUX 16
#define DLY_H 10
DI void st16(rsrc_t rs, unsigned off, unsigned so, u32x4 v) { __builtin_amdgcn_raw_buffer_store_b128(v, rs, off, so, ST_AUX); }
DI u32x4 ld16(rsrc_t rs, unsigned off, unsigned so) { return __builtin_amdgcn_raw_buffer_load_b128(rs, off, so, LD_AUX); }
DI u32x4 ld16b(rsrc_t rs, unsigned off, unsigned so) { return __builtin_amdgcn_raw_buffer_load_b128(rs, off, so, LDB_AUX); }
DI unsigned ld4(rsrc_t rs, unsigned off, unsigned so) { return __builtin_amdgcn_raw_buffer_load_b32(rs, off, so, 16); }
DI void st4(rsrc_t rs, unsigned off, unsigned so, unsigned v) { __builtin_amdgcn_raw_buffer_store_b32(v, rs, off, so, 16); }
DI unsigned xor3(unsigned a, unsigned b, unsigned c) { return __builtin_amdgcn_bitop3_b32(a, b, c, 0x96); }
DI u32x4 piece(unsigned a, unsigned b, unsigned c, unsigned tag) { u32x4 v = {a, b, c, xor3(a, b, c) ^ tag}; return v; }
DI f32x4 asf(u32x4 v) { return __builtin_bit_cast(f32x4, v); }
DI f32x4 sub4(const f32x16& x, int o) { f32x4 r = {x[o], x[o + 1], x[o + 2], x[o + 3]}; return r; }
DI u32x4 pk44(f32x4 a, f32x4 b) { u32x4 r = {pk(a[0], a[1]), pk(a[2], a[3]), pk(b[0], b[1]), pk(b[2], b[3])}; return r; }
DI u32x4 pkc(f32x4 re, f32x4 im) { u32x4 r = {pk(re[0], im[0]), pk(re[1], im[1]), pk(re[2], im[2]), pk(re[3], im[3])}; return r; }
DI rsrc_t mkrs(const void* p, unsigned bytes) { return __builtin_amdgcn_make_buffer_rsrc((void*)p, 0, bytes, 0x00020000); }
template <int NT>
DI void dma16(const void* g, unsigned lds) {
    unsigned keep;
    if (NT) asm volatile("s_mov_b32 %0, m0\n\ts_mov_b32 m0, %2\n\ts_nop 0\n\tglobal_load_lds_dwordx4 %1, off nt\n\ts_mov_b32 m0, %0" : "=&s"(keep) : "v"(g), "s"(lds) : "memory");
    else asm volatile("s_mov_b32 %0, m0\n\ts_mov_b32 m0, %2\n\ts_nop 0\n\tglobal_load_lds_dwordx4 %1, off\n\ts_mov_b32 m0, %0" : "=&s"(keep) : "v"(g), "s"(lds) : "memory");
}
#define OUTB_AUX 0

DI void fetch4(rsrc_t rs, unsigned off, unsigned stride, unsigned tag, u32x4 (&f)[3]) {
    u32x4 x[4];
#pragma unroll 1
    for (unsigned spins = 0; spins < SPIN_CAP; ++spins) {
        asm volatile("" ::: "memory");
        unsigned c[4];
#pragma unroll
        for (int t = 0; t < 4; ++t) x[t] = ld16(rs, off, t * stride);
#pragma unroll
        for (int t = 0; t < 4; ++t) c[t] = xor3(xor3(x[t][0], x[t][1], x[t][2]), x[t][3], tag);
        if (__builtin_amdgcn_ballot_w64((c[0] | c[1] | c[2] | c[3]) != 0) == 0) break;
    }
#pragma unroll
    for (int e = 0; e < 3; ++e) f[e] = u32x4{x[0][e], x[1][e], x[2][e], x[3][e]};
}

template <int CTRL, int BANK>
DI float dppo(float old, float v) {
    return __int_as_float(__builtin_amdgcn_update_dpp(__float_as_int(old), __float_as_int(v), CTRL, 0xf, BANK, false));
}
template <int CTRL>
DI float dppz(float v) { return __int_as_float(__builtin_amdgcn_update_dpp(0, __float_as_int(v), CTRL, 0xf, 0xf, true)); }
DI void reduce_tile(const f32x16& a, f32x4& o, float& c) {
    float r0 = a[0], r1 = a[1], r2 = a[2], r3 = a[3], r4 = a[4], r5 = a[5], r6 = a[6], r7 = a[7];
    float r8 = a[8], r9 = a[9], r10 = a[10], r11 = a[11], r12 = a[12], r13 = a[13], r14 = a[14], r15 = a[15];
    asm volatile(
        "s_nop 7\n\t"
        "s_nop 3\n\t"
        "v_mov_b32 %16, 0\n\t"
        "v_add_f32_dpp %0, %1, %0 row_shr:4 row_mask:0xf bank_mask:0xa\n\t"
        "v_add_f32_dpp %2, %3, %2 row_shr:4 row_mask:0xf bank_mask:0xa\n\t"
        "v_add_f32_dpp %4, %5, %4 row_shr:4 row_mask:0xf bank_mask:0xa\n\t"
        "v_add_f32_dpp %6, %7, %6 row_shr:4 row_mask:0xf bank_mask:0xa\n\t"
        "v_add_f32_dpp %8, %9, %8 row_shr:4 row_mask:0xf bank_mask:0xa\n\t"
        "v_add_f32_dpp %10, %11, %10 row_shr:4 row_mask:0xf bank_mask:0xa\n\t"
        "v_add_f32_dpp %12, %13, %12 row_shr:4 row_mask:0xf bank_mask:0xa\n\t"
        "v_add_f32_dpp %14, %15, %14 row_shr:4 row_mask:0xf bank_mask:0xa\n\t"
        "v_mov_b32_dpp %16, %15 row_shl:4 row_mask:0xf bank_mask:0x5\n\t"
        "v_add_f32_dpp %2, %1, %2 row_shl:4 row_mask:0xf bank_mask:0x5\n\t"
        "v_add_f32_dpp %6, %5, %6 row_shl:4 row_mask:0xf bank_mask:0x5\n\t"
        "v_add_f32_dpp %10, %9, %10 row_shl:4 row_mask:0xf bank_mask:0x5\n\t"
        "v_add_f32_dpp %14, %13, %14 row_shl:4 row_mask:0xf bank_mask:0x5\n\t"
        "v_add_f32_dpp %4, %3, %4 row_shl:4 row_mask:0xf bank_mask:0x5\n\t"
        "v_add_f32_dpp %8, %7, %8 row_shl:4 row_mask:0xf bank_mask:0x5\n\t"
        "v_add_f32_dpp %12, %11, %12 row_shl:4 row_mask:0xf bank_mask:0x5\n\t"
        "v_add_f32_dpp %0, %2, %0 row_ror:8 row_mask:0xf bank_mask:0xc\n\t"
        "v_add_f32_dpp %4, %6, %4 row_ror:8 row_mask:0xf bank_mask:0xc\n\t"
        "v_add_f32_dpp %8, %10, %8 row_ror:8 row_mask:0xf bank_mask:0xc\n\t"
        "v_add_f32_dpp %12, %14, %12 row_ror:8 row_mask:0xf bank_mask:0xc\n\t"
        "v_add_f32_dpp %16, %14, %16 row_ror:8 row_mask:0xf bank_mask:0x3\n\t"
        "v_add_f32_dpp %4, %2, %4 row_ror:8 row_mask:0xf bank_mask:0x3\n\t"
        "v_add_f32_dpp %8, %6, %8 row_ror:8 row_mask:0xf bank_mask:0x3\n\t"
        "v_add_f32_dpp %12, %10, %12 row_ror:8 row_mask:0xf bank_mask:0x3\n\t"
        : "+v"(r0), "+v"(r1), "+v"(r2), "+v"(r3), "+v"(r4), "+v"(r5), "+v"(r6), "+v"(r7), "+v"(r8), "+v"(r9), "+v"(r10), "+v"(r11),
          "+v"(r12), "+v"(r13), "+v"(r14), "+v"(r15), "=&v"(c));
    o = f32x4{r0, r4, r8, r12};
}

#define CODE_WGS 0
#define CODE_BYTES 21248
#define OUT_AUX 0

template <int V> struct IC { static constexpr int v = V; };
struct ConvCtx {
    int pa, pb, bz, pu, csrc, yo;
    float dv;
    bool first, diag;
};
DI float edge_sum(f32x4 d, bool diag) {
    float t = dppz<0x104>(d[3]) + dppz<0x108>(d[2]) + dppz<0x10c>(d[1]);
    t = diag ? t : 0.f;
    t = __builtin_amdgcn_mfma_f32_16x16x4f32(1.0f, t, zero4(), 0, 0, 0)[0];
    t += dppz<0xb1>(t);
    t += dppz<0x4e>(t);
    return t;
}
DI u32x2 finish_tile(const f32x16& acc, float edge, u32x2 u, const ConvCtx& c) {
    f32x4 o;
    float cy;
    reduce_tile(acc, o, cy);
    cy = __int_as_float(__builtin_amdgcn_ds_bpermute(c.csrc, __float_as_int(cy)));
    const f32x4 uf = {bfhi(u[1]), bflo(u[1]), bfhi(u[0]), bflo(u[0])};
    o = uf * c.dv + o;
    o[0] += c.first ? edge : cy;
    return u32x2{pk(o[0], o[1]), pk(o[2], o[3])};
}
template <int PART>
DI void conv(char* smem, const ConvCtx& c, const u32x4 (&ea)[4], u32x4 a0) {
    constexpr int T1 = 7 - PART, T2 = PART == 0 ? 3 : 2 * PART, T3 = 2 - PART;
    constexpr int NS = PART == 2 ? 24 : 16, PF = PART == 2 ? 2 : 1;
    constexpr int F2 = 4 * T2 + 4 + PF, F3 = PART == 0 ? -9 : (PART == 1 ? 8 + PF : NS + PF);
    const char* pa = smem + c.pa;
    const char* pb = smem + c.pb;
    u32x4 fa[32], fb[32], fc[8];
    f32x16 acc1 = zero16(), acc2 = zero16(), acc3 = zero16();
    u32x4 eb[7];
    u32x2 u1, u2, u3, y2;
    f32x4 d1 = zero4(), d2 = zero4(), d3 = zero4();
    float e1, e2, e3 = 0.f;
    fa[0] = a0;
#define LDA(m) fa[m] = trread2(pa + 128 * (m), pa + 128 * (m) + 32)
#define BFR(n) (*(const u32x4*)(pb + 128 * ((n) + 3)))
#define LDB(n) fb[(n) + 3] = BFR(n)
#define EB(T, i) (*(const u32x4*)(smem + c.bz + 512 * (T) - 1024 * (i)))
#pragma unroll
    for (int i = 0; 2 * i < T1; ++i) eb[i] = EB(T1, i);
#pragma unroll
    for (int i = 0; 2 * i < T2; ++i) eb[4 + i] = EB(T2, i);
    if (PART == 1) eb[6] = EB(1, 0);
#pragma unroll
    for (int s = 0; s < NS + PF; ++s) {
        asm volatile("" ::: "memory");
        if (s < NS) {
            if (s >= 1) LDA(s);
            if (PART == 0) { LDB(28 - s); LDB(12 - s); LDA(s + 16); }
            if (PART == 1) { LDB(24 - s); if (s < 12) { LDB(8 - s); LDA(s + 16); } }
            if (PART == 2) { if (s < 4) LDB(20 - s); if (s < 20) LDB(16 - s); }
            if (PART == 1 && s < 8) fc[s] = BFR(4 - s);
            if (PART == 2 && s >= 20) fc[s - 20] = trread2(pa + 128 * (s - 20), pa + 128 * (s - 20) + 32);
        }
        if (s == F3 - 1) u3 = *(const u32x2*)(smem + c.pu + 512 * T3);
        if (s == F2 - 1) u2 = *(const u32x2*)(smem + c.pu + 512 * T2);
        if (s == NS + PF - 1) u1 = *(const u32x2*)(smem + c.pu + 512 * T1);
        __builtin_amdgcn_sched_barrier(0);
        if (s == 0) {
#pragma unroll
            for (int i = 0; 2 * i < T1; ++i) {
                d1 = mfma16(ea[i], eb[i], d1);
                if (2 * i < T2) d2 = mfma16(ea[i], eb[4 + i], d2);
            }
            if (PART == 1) d3 = mfma16(ea[0], eb[6], d3);
        }
        if (s >= PF) {
            const int m = s - PF;
            acc1 = mfma(fa[m], fb[4 * T1 - m + 3], acc1);
            if (m < 4 * T2 + 4) acc2 = mfma(fa[m], fb[4 * T2 - m + 3], acc2);
            if (PART < 2 && m < 4 * T2 + 4) acc1 = mfma(fa[m + 16], fb[4 * T2 - m + 3], acc1);
            if (PART == 1 && m < 8) acc3 = mfma(fa[m], fc[m], acc3);
            if (PART == 2 && m >= 20) acc3 = mfma(fc[m - 20], fb[20 - m + 3], acc3);
        }
        if (s == PF) {
            e1 = edge_sum(d1, c.diag);
            e2 = edge_sum(d2, c.diag);
            if (PART == 1) e3 = edge_sum(d3, c.diag);
        }
        if (s == F3) *(u32x2*)(smem + c.yo + (LB_U + 3584 - LB_Y)) = finish_tile(acc3, e3, u3, c);
        if (s == F2) y2 = finish_tile(acc2, e2, u2, c);
        __builtin_amdgcn_sched_barrier(0);
    }
#undef LDA
#undef LDB
#undef BFR
#undef EB
    if (PART == 0) y2 = finish_tile(acc2, e2, u2, c);
    if (PART == 2) *(u32x2*)(smem + c.yo + (LB_U + 3584 - LB_Y)) = finish_tile(acc3, e3, u3, c);
    const u32x2 y1 = finish_tile(acc1, e1, u1, c);
    *(u32x2*)(smem + c.yo + 512) = y2;
    *(u32x2*)(smem + c.yo) = y1;
}

constexpr int LA_EX = 10240, LA_E1 = 28672, LA_T3 = 53248, LA_T1 = 67584;

__global__ __launch_bounds__(512, 4) void k_fused(const u16* __restrict__ x, const u16* __restrict__ wr, const u16* __restrict__ wi,
                                                  const u16* __restrict__ qq, const u16* __restrict__ kk, const u16* __restrict__ vv,
                                                  const u16* __restrict__ shk, const u16* __restrict__ hk, const u16* __restrict__ dd,
                                                  u16* __restrict__ out, char* ws) {
    __shared__ __attribute__((aligned(16))) char smem[LDS_BYTES];
    const int tid = threadIdx.x, lane = tid & 63;
    const int w = __builtin_amdgcn_readfirstlane(tid >> 6);
    const int n = lane & 31, hh = lane >> 5;
    const int g = lane >> 4, q = (lane >> 2) & 3, p = lane & 3;
    const unsigned lb = (unsigned)(size_t)(lds_vp)smem;
    STAMP_DECL
    STAMP(0);
    unsigned warm = 0;
    if (w >= 5 && blockIdx.x < CODE_WGS) {
        unsigned long long pc;
        asm volatile("s_getpc_b64 %0" : "=s"(pc));
        asm volatile("global_load_dword %0, %1, %2" : "=v"(warm) : "v"(min((tid - 320) * 128, CODE_BYTES)), "s"(pc) : "memory");
    }
    asm volatile("" :: "s"(x), "s"(wr), "s"(wi), "s"(qq), "s"(kk), "s"(vv), "s"(shk), "s"(hk), "s"(dd), "s"(out), "s"(ws));
    const rsrc_t rs = mkrs(ws, WS_END);
#define rs0 rs
    if (blockIdx.x < 256) {
        const int i2 = blockIdx.x;
        const bool hasU = i2 < 8 * 27;
        if (w < 4) {
            {
                const char* r0 = (const char*)(x + (size_t)(256 * (2 * w) + i2) * D_);
                dma16<NT_X>(r0 + lane * 16, lb + w * 2560);
                dma16<NT_X>(r0 + 256 * D_ * 2 + lane * 16, lb + w * 2560 + 1024);
                if (lane < 32) dma16<NT_X>(r0 + g * (256 * D_ * 2) + 1024 + (lane & 15) * 16, lb + w * 2560 + 2048);
            }
            unsigned ep = ld4(rs0, WS_EP, 0);
            u32x4 c1m[2];
#pragma unroll
            for (int t = 0; t < 4; ++t) {
                unsigned f = cs((float)((256 * (4 * hh + t) + i2) * (n >> 2)) * (1.0f / 2048.0f) + 0.25f * (n & 1));
                c1m[0][t] = (n & 2) ? 0u : f;
                c1m[1][t] = (n & 2) ? f : 0u;
            }
            const int pi = min(28 * w + n, PK - 1);
            int exr[3];
#pragma unroll
            for (int e = 0; e < 3; ++e) {
                int kd = min(3 * pi + e, 320);
                int m = (kd * 1639) >> 16, c = kd - 40 * m;
                int sa = 21 * m + c, sb = 355 - sa;
                exr[e] = LA_EX + (c <= 20 ? sa : sb) * 48 + 16 * hh;
                PIN(exr[e]);
            }
            PIN(c1m[0]); PIN(c1m[1]);
            int xao = w * 2560 + (g & 1) * 1024 + (8 * hh + q) * 32 + p * 8;
            int xto = w * 2560 + 2048 + (g & 1) * 256 + q * 32 + p * 8;
            int exw = LA_EX + (42 * hh + n) * 48 + 8 * w;
            int l16 = lane * 16 + 32768;
            unsigned rowoff = i2 * ROWB + pi * 16;
            unsigned x1o = (n < 28 && 28 * w + n < PK) ? WS_X1 + (i2 >> 1) * (2 * ROWB) + (i2 & 1) * 64 + (pi >> 2) * 128 + (pi & 3) * 16 + hh * (PK * 32) : 0x80000000u;
            PIN(xao); PIN(xto); PIN(exw); PIN(x1o); PIN(l16);
            STAMP(1);
            __builtin_amdgcn_s_setprio(2);
            PIN(ep);
            const unsigned tag = 2u * __builtin_amdgcn_readfirstlane(ep) + 3u;
            DRAIN();
            BAR();
            STAMP(2);
            {
                const char* xa = smem + xao;
                const char* xt = smem + xto;
                const char* tp = smem + l16 + (LA_T1 - 32768);
                u32x4 tb[12];
                u32x4 a0 = trread2(xa, xa + 128);
                tb[0] = *(const u32x4*)tp; tb[1] = *(const u32x4*)(tp + 1024);
                u32x4 a1 = trread2(xa + 512, xa + 640);
                tb[2] = *(const u32x4*)(tp + 2048); tb[3] = *(const u32x4*)(tp + 3072);
                u32x4 a2 = trread2(xt, xt + 128);
                tb[4] = *(const u32x4*)(tp + 4096); tb[5] = *(const u32x4*)(tp + 5120);
                __builtin_amdgcn_sched_barrier(0);
                f32x16 tr = mfma(a0, tb[0], zero16());
                f32x16 ti = mfma(a0, tb[1], zero16());
                __builtin_amdgcn_sched_barrier(0);
#pragma unroll
                for (int f = 11; f >= 6; --f) tb[f] = *(const u32x4*)(tp + f * 1024);
                __builtin_amdgcn_sched_barrier(0);
                tr = mfma(a1, tb[2], tr);
                ti = mfma(a1, tb[3], ti);
                tr = mfma(a2, tb[4], tr);
                ti = mfma(a2, tb[5], ti);
                f32x16 y[2];
#pragma unroll
                for (int ss = 0; ss < 2; ++ss) {
                    f32x4 r0 = sub4(tr, 8 * ss), r1 = sub4(tr, 8 * ss + 4), i0 = sub4(ti, 8 * ss), i1 = sub4(ti, 8 * ss + 4);
                    f32x4 m2 = r0 * asf(tb[10]), m3 = r1 * asf(tb[11]), m0 = i0 * asf(tb[10]), m1 = i1 * asf(tb[11]);
                    __builtin_amdgcn_sched_barrier(0);
                    m0 = r0 * asf(tb[8]) + m0; m1 = r1 * asf(tb[9]) + m1;
                    m2 = i0 * asf(tb[8]) - m2; m3 = i1 * asf(tb[9]) - m3;
                    __builtin_amdgcn_sched_barrier(0);
                    u32x4 pr = pk44(m0, m1), pi_ = pk44(m2, m3);
                    __builtin_amdgcn_sched_barrier(0);
                    y[ss] = mfma(tb[6], pr, zero16());
                    y[ss] = mfma(tb[7], pi_, y[ss]);
                }
                if (n <= 20) {
#pragma unroll
                    for (int t = 0; t < 8; ++t) {
                        u32x2 v = {pk(y[0][2 * t], y[0][2 * t + 1]), pk(y[1][2 * t], y[1][2 * t + 1])};
                        *(u32x2*)(smem + exw + 21 * ((t & 1) + 4 * (t >> 1)) * 48) = v;
                    }
                }
            }
            BAR();
            {
                const u32x4 b0 = *(const u32x4*)(smem + exr[0]), b2 = *(const u32x4*)(smem + exr[2]), b1 = *(const u32x4*)(smem + exr[1]);
                __builtin_amdgcn_sched_barrier(0);
                f32x16 o0 = mfma(c1m[0], b0, zero16());
                f32x16 o1 = mfma(c1m[0], b2, zero16());
                o0 = mfma(c1m[1], b1, o0);
                u32x4 pc[4];
#pragma unroll
                for (int gq = 0; gq < 4; ++gq)
                    pc[gq] = piece(pk(o0[4 * gq], o0[4 * gq + 1]), pk(o0[4 * gq + 2], o0[4 * gq + 3]), pk(o1[4 * gq], o1[4 * gq + 1]), tag);
#pragma unroll
                for (int gq = 0; gq < 4; ++gq) st16(rs, x1o, gq * 2 * PK * 32, pc[gq]);
            }
            STAMP(3);
            DRAIN();
            STAMP(4);
            BAR();
            u32x4 tc[14];
#pragma unroll
            for (int f = 0; f < 14; ++f) tc[f] = *(const u32x4*)(smem + l16 + (LA_T3 - 32768) + f * 1024);
            u32x4 ci[2];
#pragma unroll
            for (int t = 0; t < 4; ++t) {
                unsigned f = cs(0.25f * (n & 1) - (float)((256 * (n >> 2) + i2) * (4 * hh + t)) * (1.0f / 2048.0f));
                ci[0][t] = (n & 2) ? 0u : f;
                ci[1][t] = (n & 2) ? f : 0u;
            }
            PIN(ci[0]); PIN(ci[1]);
#pragma unroll
            for (int f = 0; f < 14; ++f) PIN(tc[f]);
            unsigned zoff = WS_Z1 + rowoff + 4 * hh * (PK * 16);
            const int k1c = min(n, 20);
            int e3o = (hh * 324 + 3 * pi) * 4, s0o = (2 * w * 324 + 160 * hh + k1c) * 4, s1o = (2 * w * 324 + 200 - 160 * hh - k1c) * 4;
            unsigned oo = ((256 * 2 * w + i2) * (2 * D_) + 16 * n + 8 * hh) * 2;
            unsigned oo2 = n < 8 ? oo + 1024 : 0x80000000u;
            PIN(zoff); PIN(e3o); PIN(s0o); PIN(s1o); PIN(oo); PIN(oo2);
            const rsrc_t ro = mkrs(out, S_ * 2 * D_ * 2);
            if (hasU) {
                BAR(); BAR(); BAR();
            } else {
#pragma unroll 1
                for (unsigned spins = 0; spins < SPIN_CAP; ++spins) {
                    __builtin_amdgcn_s_sleep(4);
                    asm volatile("" ::: "memory");
                    u32x4 v = ld16(rs, WS_Z1 + i2 * ROWB + w * 2 * PK * 16, 0);
                    if (__builtin_amdgcn_readfirstlane(v[0] ^ v[1] ^ v[2] ^ v[3]) == tag) break;
                }
            }
            STAMP(9);
            {
                u32x4 zb[3];
                fetch4(rs, zoff, PK * 16, tag, zb);
                STAMP(10);
                f32x16 o0 = mfma(ci[0], zb[0], zero16());
                f32x16 o1 = mfma(ci[0], zb[2], zero16());
                o0 = mfma(ci[1], zb[1], o0);
                unsigned* e3 = (unsigned*)(smem + e3o);
#pragma unroll
                for (int gq = 0; gq < 4; ++gq) {
                    e3[gq * 648] = pk(o0[4 * gq], o0[4 * gq + 1]);
                    e3[gq * 648 + 1] = pk(o0[4 * gq + 2], o0[4 * gq + 3]);
                    e3[gq * 648 + 2] = pk(o1[4 * gq], o1[4 * gq + 1]);
                }
            }
            BAR();
            if (i2 == 0 && tid == 0) st4(rs0, WS_EP, 0, ep + 1u);
            {
                const unsigned* s0 = (const unsigned*)(smem + s0o);
                const unsigned* s1 = (const unsigned*)(smem + s1o);
                const u32x4 f0 = {s0[0], s0[40], s0[80], s0[120]}, f1 = {s0[324], s0[364], s0[404], s0[444]};
                const u32x4 f2 = {s1[120], s1[80], s1[40], s1[0]}, f3 = {s1[444], s1[404], s1[364], s1[324]};
                __builtin_amdgcn_sched_barrier(0);
                f32x16 tr = mfma(f0, tc[0], zero16());
                f32x16 ti = mfma(f1, tc[0], zero16());
                tr = mfma(f2, tc[1], tr);
                ti = mfma(f3, tc[1], ti);
                f32x4 re[3], im[3], mr[3], mi[3];
#pragma unroll
                for (int r = 0; r < 12; ++r) {
                    auto sw = __builtin_amdgcn_permlane16_swap(__float_as_uint(tr[r]), __float_as_uint(ti[r]), false, false);
                    re[r >> 2][r & 3] = __uint_as_float(sw[0]);
                    im[r >> 2][r & 3] = __uint_as_float(sw[1]);
                }
                __builtin_amdgcn_sched_barrier(0);
#pragma unroll
                for (int j = 0; j < 3; ++j) { mr[j] = im[j] * asf(tc[11 + j]); mi[j] = re[j] * asf(tc[11 + j]); }
                __builtin_amdgcn_sched_barrier(0);
#pragma unroll
                for (int j = 0; j < 3; ++j) { mr[j] = re[j] * asf(tc[8 + j]) - mr[j]; mi[j] = im[j] * asf(tc[8 + j]) + mi[j]; }
                __builtin_amdgcn_sched_barrier(0);
                const u32x4 a0 = pk44(mr[0], mr[1]), a1 = pk44(mi[0], mi[1]), a2 = pk44(mr[2], mi[2]);
                f32x16 y0 = mfma(a0, tc[2], zero16());
                y0 = mfma(a1, tc[3], y0);
                y0 = mfma(a2, tc[4], y0);
                f32x16 y1 = mfma(a0, tc[5], zero16());
                y1 = mfma(a1, tc[6], y1);
                y1 = mfma(a2, tc[7], y1);
                __builtin_amdgcn_raw_buffer_store_b128(pack8(y0, 0), ro, oo, 0, OUT_AUX);
                __builtin_amdgcn_raw_buffer_store_b128(pack8(y0, 8), ro, oo, 256 * 2 * D_ * 2, OUT_AUX);
                __builtin_amdgcn_raw_buffer_store_b128(pack8(y1, 0), ro, oo2, 0, OUT_AUX);
                __builtin_amdgcn_raw_buffer_store_b128(pack8(y1, 8), ro, oo2, 256 * 2 * D_ * 2, OUT_AUX);
            }
            STAMP(11);
        } else {
            const int w2 = w - 4;
#pragma unroll
            for (int k = 0; k < 3; ++k) dma16<0>((const char*)g_t1.v + (3 * w2 + k) * 1024 + lane * 16, lb + LA_T1 + (3 * w2 + k) * 1024);
            const unsigned ep = ld4(rs0, WS_EP, 0);
            const int k1s = i2 & 7, pi = 4 * (i2 >> 3) + p;
            const unsigned we = (4 * w2 + q + 64 * g) * NW + k1s * DF + 3 * pi;
            unsigned vo = hasU ? (we * 2) & ~3u : 0x80000000u;
            PIN(vo);
            u32x4 fre, fim, fin;
            f32x4 tc, ts;
#pragma unroll
            for (int t = 0; t < 4; ++t) {
                float an = (float)((lane & 15) * (4 * g + t)) * (1.0f / 16.0f);
                float c = fcos(an), s = fsin(an);
                fre[t] = pk(c, s);
                fim[t] = pk(-s, c);
                fin[t] = fim[t] ^ 0x80008000u;
                float b = (float)((4 * w2 + q) * (4 * g + t)) * (1.0f / 256.0f);
                tc[t] = fcos(b); ts[t] = fsin(b);
            }
            PIN(fre); PIN(fim); PIN(fin); PIN(tc); PIN(ts);
            DRAIN();
            BAR();
            asm volatile("" :: "v"(warm));
            u32x2 wraw[4][2];
            {
                const rsrc_t rwr = mkrs(wr, S_ * DF * 2), rwi = mkrs(wi, S_ * DF * 2);
#pragma unroll
                for (int r = 0; r < 4; ++r) {
                    wraw[r][0] = __builtin_amdgcn_raw_buffer_load_b64(rwr, vo, r * 16 * NW * 2, NT_W);
                    wraw[r][1] = __builtin_amdgcn_raw_buffer_load_b64(rwi, vo, r * 16 * NW * 2, NT_W);
                }
            }
#pragma unroll
            for (int k = 0; k < 4; ++k) {
                const int f = min(4 * w2 + k, 13);
                dma16<0>((const char*)g_t3.v + f * 1024 + lane * 16, lb + LA_T3 + f * 1024);
            }
            BAR();
            DRAIN();
            f32x4 wf[2][3];
            {
                const unsigned sh = (we & 1) * 16;
#pragma unroll
                for (int r = 0; r < 4; ++r)
#pragma unroll
                    for (int a = 0; a < 2; ++a) {
                        unsigned lo = __builtin_amdgcn_alignbit(wraw[r][a][1], wraw[r][a][0], sh), hi = wraw[r][a][1] >> sh;
                        wf[a][0][r] = bflo(lo); wf[a][1][r] = bfhi(lo); wf[a][2][r] = bflo(hi);
                    }
#pragma unroll
                for (int e = 0; e < 3; ++e) { PIN(wf[0][e]); PIN(wf[1][e]); }
            }
            const unsigned tag = 2u * __builtin_amdgcn_readfirstlane(ep) + 3u;
            unsigned coloff = (64 * g + 4 * w2 + q) * ROWB + (k1s * PK + pi) * 16;
            int ewo = LA_E1 + (g * 768 + (16 * w2 + p) * 4 + q) * 4, ero = LA_E1 + w2 * 3072 + lane * 16;
            unsigned xoff = WS_X1 + (64 * g + 4 * w2 + (q & 2)) * ROWB + (k1s * 27 + (i2 >> 3)) * 128 + (lane & 7) * 16;
            PIN(coloff); PIN(ewo); PIN(ero); PIN(xoff);
            unsigned* ew = (unsigned*)(smem + ewo);
            const char* er = smem + ero;
            STAMP(1);
            __builtin_amdgcn_s_setprio(2);
            BAR();
            if (hasU) {
            STAMP(5);
            u32x4 f[3];
            f32x4 re[3], im[3], mr[3], mi[3];
            fetch4(rs, xoff, 16 * ROWB, tag, f);
            STAMP(6);
#define DFT16(FI) _Pragma("unroll") for (int e = 0; e < 3; ++e) { re[e] = mfma16(fre, f[e], zero4()); im[e] = mfma16(FI, f[e], zero4()); }
#define TWID_STORE(OFF) \
            _Pragma("unroll") for (int e = 0; e < 3; ++e) { \
                mr[e] = im[e] * ts; mi[e] = re[e] * ts; \
                mr[e] = re[e] * tc + mr[e]; mi[e] = im[e] * tc - mi[e]; \
                u32x4 v = pkc(mr[e], mi[e]); \
                _Pragma("unroll") for (int r = 0; r < 4; ++r) ew[OFF + e * 256 + r * 16] = v[r]; \
            }
            DFT16(fim);
            __builtin_amdgcn_sched_barrier(0);
            TWID_STORE(0);
            BAR();
#pragma unroll
            for (int e = 0; e < 3; ++e) f[e] = *(const u32x4*)(er + e * 1024);
            __builtin_amdgcn_sched_barrier(0);
            DFT16(fin);
            __builtin_amdgcn_sched_barrier(0);
#pragma unroll
            for (int e = 0; e < 3; ++e) {
                mr[e] = im[e] * wf[1][e]; mi[e] = re[e] * wf[1][e];
                mr[e] = re[e] * wf[0][e] + mr[e]; mi[e] = im[e] * wf[0][e] - mi[e];
                f[e] = pkc(mr[e], mi[e]);
                re[e] = mfma16(fre, f[e], zero4()); im[e] = mfma16(fim, f[e], zero4());
            }
            __builtin_amdgcn_sched_barrier(0);
            TWID_STORE(3072);
            BAR();
#pragma unroll
            for (int e = 0; e < 3; ++e) f[e] = *(const u32x4*)(er + 12288 + e * 1024);
            __builtin_amdgcn_sched_barrier(0);
            DFT16(fin);
            __builtin_amdgcn_sched_barrier(0);
#pragma unroll
            for (int e = 0; e < 3; ++e) f[e] = pkc(re[e], im[e]);
#pragma unroll
            for (int r = 0; r < 4; ++r) st16(rs, coloff, WS_Z1 + r * 16 * ROWB, piece(f[0][r], f[1][r], f[2][r], tag));
            STAMP(7);
            DRAIN();
            STAMP(8);
            BAR();
            }
        }
    } else {
        const int bb = blockIdx.x - 256;
        const int xg = bb & 7, ib = bb >> 3;
        const bool cons = ib < 30;
        const int gl = min(ib / 3, 9), part = ib - 3 * (ib / 3);
        const int ch0 = 80 * xg + 8 * gl;
        const unsigned epo = WS_EP + 256 * (1 + xg);
        unsigned ep;
        if (w < 5) {
            __builtin_amdgcn_s_setprio(1);
            const int rg = tid / 40, cp = tid - 40 * rg;
            const int chp = 80 * xg + 2 * cp, s0 = 64 * ib + 8 * rg;
            const rsrc_t rk = mkrs(kk, S_ * D_ * 2), rv = mkrs(vv, S_ * D_ * 2);
            const unsigned vo = (s0 * D_ + chp) * 2;
            const unsigned ho = s0 > 0 ? vo - 3 * D_ * 2 : 0x80000000u;
            unsigned kr[11], vr[8];
            const u32x4 sw = *(const u32x4*)(shk + chp * 4);
#pragma unroll
            for (int i = 0; i < 3; ++i) kr[i] = __builtin_amdgcn_raw_buffer_load_b32(rk, ho + i * D_ * 2, 0, NT_KV);
#pragma unroll
            for (int i = 0; i < 8; ++i) {
                kr[3 + i] = __builtin_amdgcn_raw_buffer_load_b32(rk, vo + (i % 3) * D_ * 2, (i / 3) * 3 * D_ * 2, NT_KV);
                vr[i] = __builtin_amdgcn_raw_buffer_load_b32(rv, vo + (i % 3) * D_ * 2, (i / 3) * 3 * D_ * 2, NT_KV);
            }
            ep = ld4(rs, epo, 0);
            __builtin_amdgcn_sched_barrier(0);
            STAMP(1);
            f32x2 wt[4] = {{bflo(sw[0]), bflo(sw[2])}, {bfhi(sw[0]), bfhi(sw[2])}, {bflo(sw[1]), bflo(sw[3])}, {bfhi(sw[1]), bfhi(sw[3])}};
            f32x2 kf[11], o[8];
#pragma unroll
            for (int i = 0; i < 11; ++i) {
                kf[i] = f32x2{bflo(kr[i]), bfhi(kr[i])};
            }
#pragma unroll
            for (int e = 0; e < 8; ++e) {
                f32x2 a = kf[e] * wt[3];
                a = kf[e + 1] * wt[2] + a;
                a = kf[e + 2] * wt[1] + a;
                a = kf[e + 3] * wt[0] + a;
                o[e] = a * f32x2{bflo(vr[e]), bfhi(vr[e])};
            }
#pragma unroll
            for (int c = 0; c < 2; ++c) {
                char* dst = smem + LB_ST + (c * 40 + cp) * 136 + rg * 16;
                *(u32x2*)dst = u32x2{pk(o[0][c], o[1][c]), pk(o[2][c], o[3][c])};
                *(u32x2*)(dst + 8) = u32x2{pk(o[4][c], o[5][c]), pk(o[6][c], o[7][c])};
            }
            PIN(ep);
            STAMP(2);
            BAR();
            const char* src = smem + LB_ST + ((p >> 1) * 40 + 8 * w + g) * 136 + 64 * (p & 1) + 8 * (7 - q);
            const unsigned mo = WS_KV + ((xg * 10 + 2 * w) * 32 + ib) * MSGB;
            const u32x4 u0 = trread2(src, src - 32), u1 = trread2(src + 4 * 136, src + 4 * 136 - 32);
            st16(rs, lane * 16, mo, u0);
            st16(rs, lane * 16, mo + 32 * MSGB, u1);
            STAMP(3);
            DRAIN();
            if (lane < 2) st4(rs, lane * (32 * MSGB) + 1024, mo, 2u * ep + 3u);
            __builtin_amdgcn_s_setprio(0);
            STAMP(4);
        } else {
            ep = ld4(rs, epo, 0);
            const int t3 = tid - 320;
#pragma unroll
            for (int k = 0; k < 2; ++k) {
                const int z = t3 + 192 * k, c = z / 36, o = z - 36 * c;
                if (z < 288) *(u32x4*)(smem + c * CHB + (o < 4 ? o * 16 : LB_Z + (o - 4) * 16)) = u32x4{0u, 0u, 0u, 0u};
            }
            PIN(ep);
            if (cons) {
                __builtin_amdgcn_s_sleep(DLY_H);
                const char* hp = (const char*)(hk + (size_t)ch0 * S_) + lane * 16;
#pragma unroll 1
                for (int k = w - 5; k < 32; k += 3) dma16<NT_H>(hp + k * 1024, lb + (k >> 2) * CHB + LB_H + (k & 3) * 1024);
            }
            BAR();
            DRAIN();
        }
        if (cons) {
            const unsigned tag = 2u * __builtin_amdgcn_readfirstlane(ep) + 3u;
            ConvCtx c;
            {
                const int pi = 2 * (g & 1) + (p >> 1) + 4 * (p & 1);
                const int a = (n & 3) | ((n >> 4) << 2), am = (a + 7) & 7;
                c.pa = w * CHB + 8 + 8 * (8 * hh + q + pi);
                c.pb = w * CHB + LB_U + 64 * (a - hh) + 16 * q - 384;
                c.bz = w * CHB + LB_U + 16 * q - 64 * (1 + 4 * p + g);
                c.pu = w * CHB + LB_U + 64 * a + 16 * q + 8 - 8 * hh;
                c.csrc = 4 * ((hh ? n : ((am & 3) | (q << 2) | ((am >> 2) << 4))) + 32 - 32 * hh);
                c.yo = w * CHB + LB_Y + lane * 8;
                c.dv = bflo(dd[ch0 + w]);
                c.first = (lane & 0x33) == 0;
                c.diag = g == p;
                PIN(c.pa); PIN(c.pb); PIN(c.bz); PIN(c.pu); PIN(c.csrc); PIN(c.yo); PIN(c.dv);
            }
            const int nk = (32 - 4 * part - w + 7) >> 3;
            const unsigned mo = WS_KV + ((xg * 10 + gl) * 32 + w) * MSGB;
            const unsigned m3 = (nk - 1) * 8;
            unsigned rowa, rowb;
            {
                const int T1 = 7 - part, T2 = part == 0 ? 3 : 2 * part, T3 = 2 - part;
                const int lq = (tid >> 2) & 63, nq = lq & 31;
                const int row3 = 32 * ((nq & 3) | ((nq >> 4) << 2)) + 16 * (lq >> 5) + ((nq >> 2) & 3) + 4 * p;
                rowa = (row3 + 256 * (w < 4 ? T1 : T2)) * (D_ * 2);
                rowb = (row3 + ((part > 0 && w < 4) ? 256 * T3 : 0x100000)) * (D_ * 2);
            }
            int uo = (lane >> 3) * CHB + LB_U + w * 128 + (lane & 7) * 16;
            int yso = q * CHB + LB_Y + (w * 64 + g * 16 + p * 4) * 2;
            unsigned fo = min(p, nk - 1) * 8 * MSGB + 1024;
            int bao = w * CHB + LB_H + 8 * (32 * p + 8 * g + q);
            unsigned qoa = rowa, qob = rowb;
            PIN(rowa); PIN(rowb); PIN(uo); PIN(yso); PIN(fo); PIN(bao);
            BAR();
            auto tail = [&](auto part_c) __attribute__((always_inline)) {
            constexpr int PART = decltype(part_c)::v;
            u32x4 ea[4];
#pragma unroll
            for (int i = 0; i < 4; ++i) { ea[i] = trread2(smem + bao + 1024 * i, smem + bao + 1024 * i + 32); PIN(ea[i]); }
            u32x4 a0 = trread2(smem + c.pa, smem + c.pa + 32);
            PIN(a0);
#pragma unroll 1
            for (unsigned spins = 0; spins < SPIN_CAP; ++spins) {
                asm volatile("" ::: "memory");
                unsigned fl = ld4(rs, fo, mo);
                COUNT(13);
                if (__builtin_amdgcn_ballot_w64(fl != tag) == 0) break;
                __builtin_amdgcn_s_sleep(2);
            }
            asm volatile("" ::: "memory");
            STAMP(5);
            {
                u32x4 um[4];
#pragma unroll
                for (int k = 0; k < 4; ++k) um[k] = ld16b(rs, lane * 16, mo + (k < 3 ? 8 * k : m3) * MSGB);
#pragma unroll
                for (int k = 0; k < 3; ++k) *(u32x4*)(smem + uo + 1024 * k) = um[k];
                *(u32x4*)(smem + uo + 128 * m3) = um[3];
            }
            STAMP(6);
            BAR();
            STAMP(9);
            const rsrc_t rq = mkrs(qq, S_ * D_ * 2), ro = mkrs(out, S_ * 2 * D_ * 2);
            const u32x4 qa = __builtin_amdgcn_raw_buffer_load_b128(rq, qoa, ch0 * 2, NT_Q);
            const u32x4 qb = __builtin_amdgcn_raw_buffer_load_b128(rq, qob, ch0 * 2, NT_Q);
            __builtin_amdgcn_sched_barrier(0);
            conv<PART>(smem, c, ea, a0);
            STAMP(7);
            BAR();
            {
                const char* ys = smem + yso;
                const u32x4 y = trread2(ys, ys + 4 * CHB), z = trread2(ys + (LB_U + 3584 - LB_Y), ys + (LB_U + 3584 - LB_Y) + 4 * CHB);
                u32x4 o;
#pragma unroll
                for (int k = 0; k < 4; ++k) o[k] = pk(bflo(y[k]) * bflo(qa[k]), bfhi(y[k]) * bfhi(qa[k]));
                __builtin_amdgcn_raw_buffer_store_b128(o, ro, 2 * rowa, (D_ + ch0) * 2, OUTB_AUX);
#pragma unroll
                for (int k = 0; k < 4; ++k) o[k] = pk(bflo(z[k]) * bflo(qb[k]), bfhi(z[k]) * bfhi(qb[k]));
                __builtin_amdgcn_raw_buffer_store_b128(o, ro, 2 * rowb, (D_ + ch0) * 2, OUTB_AUX);
            }
            };
            if (part == 0) tail(IC<0>{});
            else if (part == 1) tail(IC<1>{});
            else tail(IC<2>{});
            if (ib == 0 && tid == 0) st4(rs, epo, 0, ep + 1u);
            STAMP(8);
        }
    }
    STAMP_OUT;
}
extern "C" void kernel_launch(void* const* d_in, const int* in_sizes, int n_in, void* d_out, int out_size, void* d_ws,
                              size_t ws_size, hipStream_t stream) {
    k_fused<<<512, 512, 0, stream>>>((const u16*)d_in[0], (const u16*)d_in[1], (const u16*)d_in[2], (const u16*)d_in[3],
                                     (const u16*)d_in[4], (const u16*)d_in[5], (const u16*)d_in[6], (const u16*)d_in[7],
                                     (const u16*)d_in[8], (u16*)d_out, (char*)d_ws + WSOFF);
}
```

```cpp
#include <hip/hip_runtime.h>
#include <hip/hip_bf16.h>

typedef __attribute__((ext_vector_type(4))) short s16x4;
typedef __attribute__((ext_vector_type(2))) unsigned short u16x2;
typedef __attribute__((ext_vector_type(2))) __bf16 bf16x2;
typedef __attribute__((ext_vector_type(8))) __bf16 bf16x8;
typedef __attribute__((ext_vector_type(2))) float f32x2;
typedef __attribute__((ext_vector_type(4))) float f32x4;
typedef __attribute__((ext_vector_type(16))) float f32x16;
typedef __attribute__((ext_vector_type(2))) unsigned u32x2;
typedef __attribute__((ext_vector_type(4))) unsigned u32x4;
typedef __attribute__((ext_vector_type(4))) int i32x4;
typedef __attribute__((address_space(3))) s16x4* lds_s16x4p;
typedef __attribute__((address_space(3))) void* lds_vp;
typedef __attribute__((address_space(3))) unsigned lds_u32;
typedef unsigned short u16;
typedef __amdgpu_buffer_rsrc_t rsrc_t;

#define DI __device__ __forceinline__
#define LAM [&]() __attribute__((always_inline))

constexpr int S_ = 2048, D_ = 640, DF = 321;
constexpr int NW = 8 * DF;
constexpr int PK = 108;
constexpr int ROWB = 8 * PK * 16;
constexpr unsigned WS_X1 = 0, WS_Z1 = 4u << 20, WS_KV = 8u << 20, WS_CK = 11u << 20, WS_EP = WS_CK + 80 * 64 * 64, WS_END = 12u << 20;
constexpr unsigned OOB = 0x80000000u;
constexpr unsigned SPIN_CAP = 1u << 17, LDS_SPIN_CAP = 1u << 22;
constexpr int NTHR = 768;
#define DLY_T 6
#define PRE_A 8
#define PRE_Z 4
constexpr int CODE_WGS = 64, CODE_BYTES = 22912;
constexpr int CHB = 8768, LB_H = 64, LB_Y = 2112, LB_Z = 4160, LB_U = 4672, LB_ST = 8 * CHB;
constexpr int LA = 81920, LA_EX = LA + 12288, LA_T1 = LA + 28416, LA_E3 = LA_T1, LA_T3 = LA + 40704, LA_K = LA + 55040, LA_E1 = LA_K;
constexpr int LA_SY = LA_K + 12288, LA_V = LA_SY + 1024, LA_Q = LA_V, LDS_BYTES = LA_V + 12288;

DI unsigned pk(float lo, float hi) {
    f32x2 v = {lo, hi};
    return __builtin_bit_cast(unsigned, __builtin_convertvector(v, bf16x2));
}
DI float bflo(unsigned u) { return __uint_as_float(u << 16); }
DI float bfhi(unsigned u) { return __uint_as_float(u & 0xffff0000u); }
DI f32x16 mfma(u32x4 a, u32x4 b, f32x16 c) {
    return __builtin_amdgcn_mfma_f32_32x32x16_bf16(__builtin_bit_cast(bf16x8, a), __builtin_bit_cast(bf16x8, b), c, 0, 0, 0);
}
DI f32x4 mfma16(u32x4 a, u32x4 b, f32x4 c) {
    return __builtin_amdgcn_mfma_f32_16x16x32_bf16(__builtin_bit_cast(bf16x8, a), __builtin_bit_cast(bf16x8, b), c, 0, 0, 0);
}
DI float fsin(float rev) { return __builtin_amdgcn_sinf(rev); }
DI float fcos(float rev) { return __builtin_amdgcn_cosf(rev); }
DI unsigned cs(float rev) { return pk(fcos(rev), fsin(rev)); }
DI u32x4 trread2(const char* p0, const char* p1) {
    s16x4 a = __builtin_amdgcn_ds_read_tr16_b64_v4i16((lds_s16x4p)p0);
    s16x4 b = __builtin_amdgcn_ds_read_tr16_b64_v4i16((lds_s16x4p)p1);
    return __builtin_bit_cast(u32x4, __builtin_shufflevector(a, b, 0, 1, 2, 3, 4, 5, 6, 7));
}
DI u32x4 pack8(const f32x16& x, int o) {
    u32x4 r = {pk(x[o], x[o + 1]), pk(x[o + 2], x[o + 3]), pk(x[o + 4], x[o + 5]), pk(x[o + 6], x[o + 7])};
    return r;
}
DI f32x16 zero16() { f32x16 z = {0.f, 0.f, 0.f, 0.f, 0.f, 0.f, 0.f, 0.f, 0.f, 0.f, 0.f, 0.f, 0.f, 0.f, 0.f, 0.f}; return z; }
DI f32x4 zero4() { f32x4 z = {0.f, 0.f, 0.f, 0.f}; return z; }
DI f32x4 asf(u32x4 v) { return __builtin_bit_cast(f32x4, v); }
DI f32x4 sub4(const f32x16& x, int o) { f32x4 r = {x[o], x[o + 1], x[o + 2], x[o + 3]}; return r; }
DI u32x4 pk44(f32x4 a, f32x4 b) { u32x4 r = {pk(a[0], a[1]), pk(a[2], a[3]), pk(b[0], b[1]), pk(b[2], b[3])}; return r; }
DI u32x4 pkc(f32x4 re, f32x4 im) { u32x4 r = {pk(re[0], im[0]), pk(re[1], im[1]), pk(re[2], im[2]), pk(re[3], im[3])}; return r; }
template <int CTRL>
DI unsigned dppu(unsigned v) { return __builtin_amdgcn_update_dpp(0, v, CTRL, 0xf, 0xf, true); }
template <int CTRL>
DI float dppz(float v) { return __int_as_float(__builtin_amdgcn_update_dpp(0, __float_as_int(v), CTRL, 0xf, 0xf, true)); }
#define PIN(x) asm volatile("" : "+v"(x))
#define FENCE() __builtin_amdgcn_sched_barrier(0)
#define VMWAIT(n) asm volatile("s_waitcnt vmcnt(" #n ")" ::: "memory")

DI rsrc_t mkrs(const void* p, unsigned bytes) { return __builtin_amdgcn_make_buffer_rsrc((void*)p, 0, bytes, 0x00020000); }
DI i32x4 rawrs(const void* p, unsigned bytes) {
    const unsigned long long a = (unsigned long long)p;
    return i32x4{(int)(unsigned)a, (int)((unsigned)(a >> 32) & 0xffffu), (int)bytes, 0x00020000};
}
template <int AUX>
DI void st16(rsrc_t rs, unsigned off, unsigned so, u32x4 v) {
    __builtin_amdgcn_raw_buffer_store_b128(v, rs, off, so, AUX);
    asm volatile("s_nop 1" :: "v"(v) : "memory");
}
DI u32x4 ld16(rsrc_t rs, unsigned off, unsigned so) { return __builtin_amdgcn_raw_buffer_load_b128(rs, off, so, 16); }
DI unsigned ld4(rsrc_t rs, unsigned off, unsigned so) { return __builtin_amdgcn_raw_buffer_load_b32(rs, off, so, 16); }
DI void st4(rsrc_t rs, unsigned off, unsigned so, unsigned v) { __builtin_amdgcn_raw_buffer_store_b32(v, rs, off, so, 16); }
DI void dma1(i32x4 rs, unsigned voff, unsigned soff, unsigned lds) {
    unsigned keep;
    asm volatile("s_mov_b32 %0, m0\n\ts_mov_b32 m0, %4\n\ts_nop 0\n\tbuffer_load_dwordx4 %1, %2, %3 offen lds\n\ts_mov_b32 m0, %0"
                 : "=&s"(keep) : "v"(voff), "s"(rs), "s"(soff), "s"(lds) : "memory");
}
DI void dma4(i32x4 rs, unsigned l16, unsigned l3, unsigned soff, unsigned lds) {
    unsigned keep;
    asm volatile("s_mov_b32 %0, m0\n\ts_mov_b32 m0, %4\n\ts_nop 0\n\t"
                 "buffer_load_dwordx4 %1, %3, %5 offen lds\n\tbuffer_load_dwordx4 %1, %3, %5 offen offset:1024 lds\n\t"
                 "buffer_load_dwordx4 %1, %3, %5 offen offset:2048 lds\n\tbuffer_load_dwordx4 %2, %3, %5 offen offset:3072 lds\n\ts_mov_b32 m0, %0"
                 : "=&s"(keep) : "v"(l16), "v"(l3), "s"(rs), "s"(lds), "s"(soff) : "memory");
}

DI unsigned xor3(unsigned a, unsigned b, unsigned c) { return __builtin_amdgcn_bitop3_b32(a, b, c, 0x96); }
DI u32x4 piece(unsigned a, unsigned b, unsigned c, unsigned tag) { u32x4 v = {a, b, c, xor3(a, b, c) ^ tag}; return v; }
DI void issue4(rsrc_t rs, unsigned off, unsigned stride, u32x4 (&x)[4]) {
#pragma unroll
    for (int t = 0; t < 4; ++t) x[t] = ld16(rs, off, t * stride);
}
DI void settle4(rsrc_t rs, unsigned off, unsigned stride, unsigned tag, u32x4 (&x)[4], u32x4 (&f)[3]) {
#pragma unroll 1
    for (unsigned spins = 0; spins < SPIN_CAP; ++spins) {
        unsigned c[4];
#pragma unroll
        for (int t = 0; t < 4; ++t) c[t] = xor3(xor3(x[t][0], x[t][1], x[t][2]), x[t][3], tag);
        if (__builtin_amdgcn_ballot_w64((c[0] | c[1] | c[2] | c[3]) != 0) == 0) break;
        __builtin_amdgcn_s_sleep(2);
        asm volatile("" ::: "memory");
#pragma unroll
        for (int t = 0; t < 4; ++t) {
            c[t] |= dppu<0xb1>(c[t]);
            c[t] |= dppu<0x4e>(c[t]);
        }
#pragma unroll
        for (int t = 0; t < 4; ++t)
            if (c[t] != 0) x[t] = ld16(rs, off, t * stride);
    }
    __builtin_amdgcn_s_waitcnt(0x0f70);
#pragma unroll
    for (int e = 0; e < 3; ++e) f[e] = u32x4{x[0][e], x[1][e], x[2][e], x[3][e]};
}
DI unsigned rowhash(u32x4 v, unsigned coef) {
    unsigned h = 0;
#pragma unroll
    for (int k = 0; k < 4; ++k) {
        const unsigned e = v[k];
        h = __builtin_amdgcn_udot2(__builtin_bit_cast(u16x2, e), __builtin_bit_cast(u16x2, coef), h, false);
    }
    h += dppu<0x128>(h); h += dppu<0x124>(h); h += dppu<0x122>(h); h += dppu<0x121>(h);
    return h;
}

enum { EV_T1, EV_K, EV_X, EV_I, EV_S, EV_P2A, EV_P2B, EV_U, EV_Y, EV_T3, EV_P3, EV_TAG };
DI void arrive(char* smem, int ev, int w, int lane) {
    asm volatile("" ::: "memory");
    if (lane == 0) __hip_atomic_store((lds_u32*)(lds_vp)(smem + LA_SY) + ev * 16 + w, 1u, __ATOMIC_RELAXED, __HIP_MEMORY_SCOPE_WORKGROUP);
    asm volatile("" ::: "memory");
}
template <class F>
DI bool probe(char* smem, int ev, unsigned mask, int syo, F& loads) {
    asm volatile("" ::: "memory");
    const unsigned v = __hip_atomic_load((lds_u32*)(lds_vp)(smem + syo) + ev * 16, __ATOMIC_RELAXED, __HIP_MEMORY_SCOPE_WORKGROUP);
    asm volatile("" ::: "memory");
    FENCE();
    loads();
    FENCE();
    return ((unsigned)__builtin_amdgcn_ballot_w64(v == 0) & mask) == 0;
}
template <int SLEEP, bool PEEL = false, class F>
DI void await(char* smem, int ev, unsigned mask, int syo, F loads) {
    if (!PEEL || !probe(smem, ev, mask, syo, loads)) {
#pragma unroll 1
        for (unsigned spins = 0; spins < LDS_SPIN_CAP; ++spins) {
            if (probe(smem, ev, mask, syo, loads)) break;
            if (SLEEP) __builtin_amdgcn_s_sleep(SLEEP);
        }
    }
    asm volatile("" ::: "memory");
}

struct ConvCtx {
    int pa, pb, ba, bz, pu, csrc, yo;
    float dv;
    bool first, diag, p0;
};
struct Fin { float o[4]; u32x2 u; float cy; };
template <int LO, int HI>
DI void fin_part(Fin& f, const f32x16& a, char* smem, const ConvCtx& c, int uoff) {
#define FS(i, stmt) if (LO <= i && i < HI) { stmt; }
    FS(0, f.cy = dppz<0x104>(a[15]); f.u = *(const u32x2*)(smem + c.pu + uoff))
    FS(1, f.o[0] = a[0] + dppz<0x114>(a[1]))   FS(2, f.o[1] = a[4] + dppz<0x114>(a[5]))
    FS(3, f.cy += dppz<0x108>(a[14]))
    FS(4, f.o[2] = a[8] + dppz<0x114>(a[9]))   FS(5, f.o[3] = a[12] + dppz<0x114>(a[13]))
    FS(6, f.cy += dppz<0x10c>(a[13]); f.cy = __int_as_float(__builtin_amdgcn_ds_bpermute(c.csrc, __float_as_int(f.cy))))
    FS(7, f.o[0] += dppz<0x118>(a[2]))   FS(8, f.o[1] += dppz<0x118>(a[6]))   FS(9, f.o[2] += dppz<0x118>(a[10]))  FS(10, f.o[3] += dppz<0x118>(a[14]))
    FS(11, f.o[0] += dppz<0x11c>(a[3]))  FS(12, f.o[1] += dppz<0x11c>(a[7]))  FS(13, f.o[2] += dppz<0x11c>(a[11])) FS(14, f.o[3] += dppz<0x11c>(a[15]))
    FS(15, f.o[1] += dppz<0x104>(a[3]))  FS(16, f.o[2] += dppz<0x104>(a[7]))  FS(17, f.o[3] += dppz<0x104>(a[11]))
    FS(18, f.o[1] += dppz<0x108>(a[2]))  FS(19, f.o[2] += dppz<0x108>(a[6]))  FS(20, f.o[3] += dppz<0x108>(a[10]))
    FS(21, f.o[1] += dppz<0x10c>(a[1]))  FS(22, f.o[2] += dppz<0x10c>(a[5]))  FS(23, f.o[3] += dppz<0x10c>(a[9]))
#undef FS
    if (HI < 24) {
        if (HI <= 6) PIN(f.cy);
        if (HI > 1) PIN(f.o[0]);
        if (HI > 2) PIN(f.o[1]);
        if (HI > 4) PIN(f.o[2]);
        if (HI > 5) PIN(f.o[3]);
    }
}
DI void fin_end(const Fin& f, float edge, char* smem, const ConvCtx& c, int yoff) {
    *(u32x2*)(smem + c.yo + yoff) = u32x2{pk(f.o[0] + (c.first ? edge : f.cy) + c.dv * bfhi(f.u[1]), f.o[1] + c.dv * bflo(f.u[1])),
                                          pk(f.o[2] + c.dv * bfhi(f.u[0]), f.o[3] + c.dv * bflo(f.u[0]))};
}
#define AFR(m) trread2(smem + c.pa + 128 * (m), smem + c.pa + 128 * (m) + 32)
#define BFR(n) (*(const u32x4*)(smem + c.pb + 128 * ((n) + 3)))
#define EB(o) (*(const u32x4*)(smem + c.bz + (o)))
#define SB asm volatile("" ::: "memory"); __builtin_amdgcn_sched_barrier(0);
#define XROW16(v) do { auto s_ = __builtin_amdgcn_permlane16_swap(__float_as_uint(v), __float_as_uint(v), false, false); v = __uint_as_float(s_[0]) + __uint_as_float(s_[1]); } while (0)
#define XROW32(v) do { auto s_ = __builtin_amdgcn_permlane32_swap(__float_as_uint(v), __float_as_uint(v), false, false); v = __uint_as_float(s_[0]) + __uint_as_float(s_[1]); } while (0)
template <int PART, class H>
DI void conv(char* smem, const ConvCtx& c, H pause) {
    constexpr int Y3 = LB_U + 3584 - LB_Y;
    f32x16 acc1, acc2, acc3;
    u32x4 ea0, ea1, ea2, ea3, eb0, eb1, eb2, eb3, ec0, ec1;
    f32x4 d1 = zero4(), d2 = zero4(), d3 = zero4();
    float e1, e2, e3, es;
    Fin g1, g2, g3;
    if constexpr (PART == 0) {
        const u32x4 f1 = AFR(0); const u32x4 f2 = BFR(12); const u32x4 f3 = BFR(28); const u32x4 f4 = AFR(16); const u32x4 f5 = AFR(1); const u32x4 f6 = BFR(11); const u32x4 f7 = BFR(27); SB
        acc2 = mfma(f1, f2, zero16()); SB const u32x4 f8 = AFR(17); SB
        acc1 = mfma(f1, f3, zero16()); SB const u32x4 f9 = AFR(2); const u32x4 f10 = BFR(10); SB
        acc1 = mfma(f4, f2, acc1); SB const u32x4 f11 = BFR(26); ea0 = trread2(smem + c.ba + 0, smem + c.ba + 32); SB
        acc2 = mfma(f5, f6, acc2); SB const u32x4 f12 = AFR(18); eb0 = EB(3584); ec0 = EB(1536); SB
        acc1 = mfma(f5, f7, acc1); SB const u32x4 f13 = AFR(3); const u32x4 f14 = BFR(9); ea1 = trread2(smem + c.ba + 1024, smem + c.ba + 1056); SB
        acc1 = mfma(f8, f6, acc1); SB const u32x4 f15 = BFR(25); eb1 = EB(2560); ec1 = EB(512); SB
        acc2 = mfma(f9, f10, acc2); SB const u32x4 f16 = AFR(19); ea2 = trread2(smem + c.ba + 2048, smem + c.ba + 2080); SB
        acc1 = mfma(f9, f11, acc1); SB const u32x4 f17 = AFR(4); const u32x4 f18 = BFR(8); eb2 = EB(1536); pause(); SB
        acc1 = mfma(f12, f10, acc1); SB const u32x4 f19 = BFR(24); d1 = mfma16(ea0, eb0, d1); d2 = mfma16(ea0, ec0, d2); ea3 = trread2(smem + c.ba + 3072, smem + c.ba + 3104); SB
        acc2 = mfma(f13, f14, acc2); SB const u32x4 f20 = AFR(20); eb3 = EB(512); SB
        acc1 = mfma(f13, f15, acc1); SB const u32x4 f21 = AFR(5); const u32x4 f22 = BFR(7); d1 = mfma16(ea1, eb1, d1); d2 = mfma16(ea1, ec1, d2); SB
        acc1 = mfma(f16, f14, acc1); SB const u32x4 f23 = BFR(23); SB
        acc2 = mfma(f17, f18, acc2); SB const u32x4 f24 = AFR(21); d1 = mfma16(ea2, eb2, d1); SB
        acc1 = mfma(f17, f19, acc1); SB const u32x4 f25 = AFR(6); const u32x4 f26 = BFR(6); SB
        acc1 = mfma(f20, f18, acc1); SB const u32x4 f27 = BFR(22); d1 = mfma16(ea3, eb3, d1); SB
        acc2 = mfma(f21, f22, acc2); SB const u32x4 f28 = AFR(22); e2 = dppz<0x104>(d2[3]) + dppz<0x108>(d2[2]); PIN(e2); SB
        acc1 = mfma(f21, f23, acc1); SB const u32x4 f29 = AFR(7); const u32x4 f30 = BFR(5); e2 += dppz<0x10c>(d2[1]); e2 = c.diag ? e2 : 0.f; PIN(e2); SB
        acc1 = mfma(f24, f22, acc1); SB const u32x4 f31 = BFR(21); e1 = dppz<0x104>(d1[3]) + dppz<0x108>(d1[2]); PIN(e1); SB
        acc2 = mfma(f25, f26, acc2); SB const u32x4 f32 = AFR(23); e1 += dppz<0x10c>(d1[1]); e1 = c.diag ? e1 : 0.f; PIN(e1); SB
        acc1 = mfma(f25, f27, acc1); SB const u32x4 f33 = AFR(8); const u32x4 f34 = BFR(4); e1 += dppz<0xb1>(e1); PIN(e1); e2 += dppz<0xb1>(e2); PIN(e2); SB
        acc1 = mfma(f28, f26, acc1); SB const u32x4 f35 = AFR(24); e1 += dppz<0x4e>(e1); PIN(e1); e2 += dppz<0x4e>(e2); PIN(e2); SB
        acc2 = mfma(f29, f30, acc2); SB const u32x4 f36 = AFR(9); const u32x4 f37 = BFR(3); es = c.p0 ? e1 : e2; XROW16(es); PIN(es); SB
        acc1 = mfma(f29, f31, acc1); SB const u32x4 f38 = AFR(25); XROW32(es); PIN(es); SB
        acc1 = mfma(f32, f30, acc1); SB const u32x4 f39 = AFR(10); const u32x4 f40 = BFR(2); e1 = es; e2 = dppz<0x55>(es); PIN(e2); SB
        acc2 = mfma(f33, f34, acc2); SB const u32x4 f41 = AFR(26); SB
        acc1 = mfma(f35, f34, acc1); SB const u32x4 f42 = AFR(11); const u32x4 f43 = BFR(1); SB
        acc2 = mfma(f36, f37, acc2); SB const u32x4 f44 = AFR(27); SB
        acc1 = mfma(f38, f37, acc1); SB const u32x4 f45 = AFR(12); const u32x4 f46 = BFR(0); SB
        acc2 = mfma(f39, f40, acc2); SB const u32x4 f47 = AFR(28); SB
        acc1 = mfma(f41, f40, acc1); SB const u32x4 f48 = AFR(13); const u32x4 f49 = BFR(-1); SB
        acc2 = mfma(f42, f43, acc2); SB const u32x4 f50 = AFR(29); SB
        acc1 = mfma(f44, f43, acc1); SB const u32x4 f51 = AFR(14); const u32x4 f52 = BFR(-2); SB
        acc2 = mfma(f45, f46, acc2); SB const u32x4 f53 = AFR(30); SB
        acc1 = mfma(f47, f46, acc1); SB const u32x4 f54 = AFR(15); const u32x4 f55 = BFR(-3); SB
        acc2 = mfma(f48, f49, acc2); SB const u32x4 f56 = AFR(31); SB
        acc1 = mfma(f50, f49, acc1); SB const u32x4 f57 = BFR(20); SB
        acc2 = mfma(f51, f52, acc2); SB const u32x4 f58 = BFR(19); SB
        acc1 = mfma(f53, f52, acc1); SB const u32x4 f59 = BFR(18); SB
        acc2 = mfma(f54, f55, acc2); SB const u32x4 f60 = BFR(17); SB
        acc1 = mfma(f56, f55, acc1); SB const u32x4 f61 = BFR(16); SB
        acc1 = mfma(f33, f57, acc1); SB const u32x4 f62 = BFR(15); fin_part<0, 3>(g2, acc2, smem, c, 1536); SB
        acc1 = mfma(f36, f58, acc1); SB const u32x4 f63 = BFR(14); fin_part<3, 6>(g2, acc2, smem, c, 1536); SB
        acc1 = mfma(f39, f59, acc1); SB const u32x4 f64 = BFR(13); fin_part<6, 9>(g2, acc2, smem, c, 1536); SB
        acc1 = mfma(f42, f60, acc1); SB  fin_part<9, 12>(g2, acc2, smem, c, 1536); SB
        acc1 = mfma(f45, f61, acc1); SB  fin_part<12, 15>(g2, acc2, smem, c, 1536); SB
        acc1 = mfma(f48, f62, acc1); SB  fin_part<15, 18>(g2, acc2, smem, c, 1536); SB
        acc1 = mfma(f51, f63, acc1); SB  fin_part<18, 21>(g2, acc2, smem, c, 1536); SB
        acc1 = mfma(f54, f64, acc1); SB  fin_part<21, 24>(g2, acc2, smem, c, 1536); fin_end(g2, e2, smem, c, 512); SB
        fin_part<0, 24>(g1, acc1, smem, c, 3584);
        fin_end(g1, e1, smem, c, 0);
    }
    if constexpr (PART == 1) {
        const u32x4 f1 = AFR(0); const u32x4 f2 = BFR(4); const u32x4 f3 = BFR(8); const u32x4 f4 = BFR(24); const u32x4 f5 = AFR(16); const u32x4 f6 = AFR(1); const u32x4 f7 = BFR(3); SB
        acc3 = mfma(f1, f2, zero16()); SB const u32x4 f8 = BFR(7); SB
        acc2 = mfma(f1, f3, zero16()); SB const u32x4 f9 = BFR(23); SB
        acc1 = mfma(f1, f4, zero16()); SB const u32x4 f10 = AFR(17); ea0 = trread2(smem + c.ba + 0, smem + c.ba + 32); SB
        acc1 = mfma(f5, f3, acc1); SB const u32x4 f11 = AFR(2); const u32x4 f12 = BFR(2); eb0 = EB(3072); ec0 = EB(512); SB
        acc3 = mfma(f6, f7, acc3); SB const u32x4 f13 = BFR(6); ea1 = trread2(smem + c.ba + 1024, smem + c.ba + 1056); SB
        acc2 = mfma(f6, f8, acc2); SB const u32x4 f14 = BFR(22); eb1 = EB(2048); SB
        acc1 = mfma(f6, f9, acc1); SB const u32x4 f15 = AFR(18); ea2 = trread2(smem + c.ba + 2048, smem + c.ba + 2080); SB
        acc1 = mfma(f10, f8, acc1); SB const u32x4 f16 = AFR(3); const u32x4 f17 = BFR(1); eb2 = EB(1024); pause(); SB
        acc3 = mfma(f11, f12, acc3); SB const u32x4 f18 = BFR(5); d1 = mfma16(ea0, eb0, d1); d3 = mfma16(ea0, ec0, d3); SB
        acc2 = mfma(f11, f13, acc2); SB const u32x4 f19 = BFR(21); SB
        acc1 = mfma(f11, f14, acc1); SB const u32x4 f20 = AFR(19); d1 = mfma16(ea1, eb1, d1); SB
        acc1 = mfma(f15, f13, acc1); SB const u32x4 f21 = AFR(4); const u32x4 f22 = BFR(0); SB
        acc3 = mfma(f16, f17, acc3); SB const u32x4 f23 = BFR(4); d1 = mfma16(ea2, eb2, d1); SB
        acc2 = mfma(f16, f18, acc2); SB const u32x4 f24 = BFR(20); e3 = dppz<0x104>(d3[3]) + dppz<0x108>(d3[2]); PIN(e3); SB
        acc1 = mfma(f16, f19, acc1); SB const u32x4 f25 = AFR(20); e3 += dppz<0x10c>(d3[1]); e3 = c.diag ? e3 : 0.f; PIN(e3); SB
        acc1 = mfma(f20, f18, acc1); SB const u32x4 f26 = AFR(5); const u32x4 f27 = BFR(-1); e1 = dppz<0x104>(d1[3]) + dppz<0x108>(d1[2]); PIN(e1); SB
        acc3 = mfma(f21, f22, acc3); SB const u32x4 f28 = BFR(3); e1 += dppz<0x10c>(d1[1]); e1 = c.diag ? e1 : 0.f; PIN(e1); SB
        acc2 = mfma(f21, f23, acc2); SB const u32x4 f29 = BFR(19); e1 += dppz<0xb1>(e1); PIN(e1); e3 += dppz<0xb1>(e3); PIN(e3); SB
        acc1 = mfma(f21, f24, acc1); SB const u32x4 f30 = AFR(21); e1 += dppz<0x4e>(e1); PIN(e1); e3 += dppz<0x4e>(e3); PIN(e3); SB
        acc1 = mfma(f25, f23, acc1); SB const u32x4 f31 = AFR(6); const u32x4 f32 = BFR(-2); es = c.p0 ? e1 : e3; XROW16(es); PIN(es); SB
        acc3 = mfma(f26, f27, acc3); SB const u32x4 f33 = BFR(2); XROW32(es); PIN(es); SB
        acc2 = mfma(f26, f28, acc2); SB const u32x4 f34 = BFR(18); e1 = es; e3 = dppz<0x55>(es); PIN(e3); SB
        acc1 = mfma(f26, f29, acc1); SB const u32x4 f35 = AFR(22); SB
        acc1 = mfma(f30, f28, acc1); SB const u32x4 f36 = AFR(7); const u32x4 f37 = BFR(-3); SB
        acc3 = mfma(f31, f32, acc3); SB const u32x4 f38 = BFR(1); SB
        acc2 = mfma(f31, f33, acc2); SB const u32x4 f39 = BFR(17); SB
        acc1 = mfma(f31, f34, acc1); SB const u32x4 f40 = AFR(23); SB
        acc1 = mfma(f35, f33, acc1); SB const u32x4 f41 = AFR(8); SB
        acc3 = mfma(f36, f37, acc3); SB const u32x4 f42 = AFR(24); SB
        acc2 = mfma(f36, f38, acc2); SB const u32x4 f43 = AFR(9); SB
        acc1 = mfma(f36, f39, acc1); SB const u32x4 f44 = AFR(25); fin_part<0, 3>(g3, acc3, smem, c, 512); SB
        acc1 = mfma(f40, f38, acc1); SB const u32x4 f45 = AFR(10); fin_part<3, 6>(g3, acc3, smem, c, 512); SB
        acc2 = mfma(f41, f22, acc2); SB const u32x4 f46 = AFR(26); fin_part<6, 9>(g3, acc3, smem, c, 512); SB
        acc1 = mfma(f42, f22, acc1); SB const u32x4 f47 = AFR(11); fin_part<9, 12>(g3, acc3, smem, c, 512); SB
        acc2 = mfma(f43, f27, acc2); SB const u32x4 f48 = AFR(27); fin_part<12, 15>(g3, acc3, smem, c, 512); SB
        acc1 = mfma(f44, f27, acc1); SB const u32x4 f49 = BFR(16); fin_part<15, 18>(g3, acc3, smem, c, 512); SB
        acc2 = mfma(f45, f32, acc2); SB const u32x4 f50 = BFR(15); fin_part<18, 21>(g3, acc3, smem, c, 512); SB
        acc1 = mfma(f46, f32, acc1); SB const u32x4 f51 = BFR(14); fin_part<21, 24>(g3, acc3, smem, c, 512); SB
        acc2 = mfma(f47, f37, acc2); SB const u32x4 f52 = BFR(13); fin_end(g3, e3, smem, c, Y3); SB
        acc1 = mfma(f48, f37, acc1); SB const u32x4 f53 = AFR(12); const u32x4 f54 = BFR(12); SB
        acc1 = mfma(f41, f49, acc1); SB const u32x4 f55 = AFR(13); const u32x4 f56 = BFR(11); fin_part<0, 3>(g2, acc2, smem, c, 1024); SB
        acc1 = mfma(f43, f50, acc1); SB const u32x4 f57 = AFR(14); const u32x4 f58 = BFR(10); fin_part<3, 6>(g2, acc2, smem, c, 1024); SB
        acc1 = mfma(f45, f51, acc1); SB const u32x4 f59 = AFR(15); const u32x4 f60 = BFR(9); fin_part<6, 9>(g2, acc2, smem, c, 1024); SB
        acc1 = mfma(f47, f52, acc1); SB  fin_part<9, 12>(g2, acc2, smem, c, 1024); SB
        acc1 = mfma(f53, f54, acc1); SB  fin_part<12, 15>(g2, acc2, smem, c, 1024); SB
        acc1 = mfma(f55, f56, acc1); SB  fin_part<15, 18>(g2, acc2, smem, c, 1024); SB
        acc1 = mfma(f57, f58, acc1); SB  fin_part<18, 21>(g2, acc2, smem, c, 1024); SB
        acc1 = mfma(f59, f60, acc1); SB  fin_part<21, 24>(g2, acc2, smem, c, 1024); fin_end(g2, g3.cy, smem, c, 512); SB
        fin_part<0, 24>(g1, acc1, smem, c, 3072);
        fin_end(g1, e1, smem, c, 0);
    }
    if constexpr (PART == 2) {
        const u32x4 f1 = AFR(0); const u32x4 f2 = BFR(0); const u32x4 f3 = AFR(1); const u32x4 f4 = BFR(-1); const u32x4 f5 = AFR(2); const u32x4 f6 = BFR(-2); const u32x4 f7 = AFR(3); const u32x4 f8 = BFR(-3); const u32x4 f9 = BFR(16); SB
        acc3 = mfma(f1, f2, zero16()); SB const u32x4 f10 = BFR(20); SB
        acc3 = mfma(f3, f4, acc3); SB const u32x4 f11 = BFR(15); SB
        acc3 = mfma(f5, f6, acc3); SB const u32x4 f12 = BFR(19); SB
        acc3 = mfma(f7, f8, acc3); SB const u32x4 f13 = BFR(14); SB
        acc2 = mfma(f1, f9, zero16()); SB const u32x4 f14 = BFR(18); SB
        acc1 = mfma(f1, f10, zero16()); SB const u32x4 f15 = BFR(13); fin_part<0, 3>(g3, acc3, smem, c, 0); SB
        acc2 = mfma(f3, f11, acc2); SB const u32x4 f16 = BFR(17); fin_part<3, 6>(g3, acc3, smem, c, 0); SB
        acc1 = mfma(f3, f12, acc1); SB const u32x4 f17 = AFR(4); const u32x4 f18 = BFR(12); fin_part<6, 9>(g3, acc3, smem, c, 0); pause(); SB
        acc2 = mfma(f5, f13, acc2); SB  fin_part<9, 12>(g3, acc3, smem, c, 0); SB
        acc1 = mfma(f5, f14, acc1); SB const u32x4 f19 = AFR(5); const u32x4 f20 = BFR(11); fin_part<12, 15>(g3, acc3, smem, c, 0); SB
        acc2 = mfma(f7, f15, acc2); SB  fin_part<15, 18>(g3, acc3, smem, c, 0); SB
        acc1 = mfma(f7, f16, acc1); SB const u32x4 f21 = AFR(6); const u32x4 f22 = BFR(10); fin_part<18, 21>(g3, acc3, smem, c, 0); SB
        acc2 = mfma(f17, f18, acc2); SB  fin_part<21, 24>(g3, acc3, smem, c, 0); SB
        acc1 = mfma(f17, f9, acc1); SB const u32x4 f23 = AFR(7); const u32x4 f24 = BFR(9); fin_end(g3, 0.f, smem, c, Y3); SB
        acc2 = mfma(f19, f20, acc2); SB  SB
        acc1 = mfma(f19, f11, acc1); SB const u32x4 f25 = AFR(8); const u32x4 f26 = BFR(8); SB
        acc2 = mfma(f21, f22, acc2); SB  ea0 = trread2(smem + c.ba + 0, smem + c.ba + 32); SB
        acc1 = mfma(f21, f13, acc1); SB const u32x4 f27 = AFR(9); const u32x4 f28 = BFR(7); eb0 = EB(2048); SB
        acc2 = mfma(f23, f24, acc2); SB  ea1 = trread2(smem + c.ba + 1024, smem + c.ba + 1056); SB
        acc1 = mfma(f23, f15, acc1); SB const u32x4 f29 = AFR(10); const u32x4 f30 = BFR(6); eb1 = EB(1024); SB
        acc2 = mfma(f25, f26, acc2); SB  SB
        acc1 = mfma(f25, f18, acc1); SB const u32x4 f31 = AFR(11); const u32x4 f32 = BFR(5); SB
        acc2 = mfma(f27, f28, acc2); SB  d2 = mfma16(ea0, eb0, d2); SB
        acc1 = mfma(f27, f20, acc1); SB const u32x4 f33 = AFR(12); const u32x4 f34 = BFR(4); SB
        acc2 = mfma(f29, f30, acc2); SB  d2 = mfma16(ea1, eb1, d2); SB
        acc1 = mfma(f29, f22, acc1); SB const u32x4 f35 = AFR(13); const u32x4 f36 = BFR(3); e2 = dppz<0x104>(d2[3]) + dppz<0x108>(d2[2]); PIN(e2); SB
        acc2 = mfma(f31, f32, acc2); SB  e2 += dppz<0x10c>(d2[1]); e2 = c.diag ? e2 : 0.f; PIN(e2); SB
        acc1 = mfma(f31, f24, acc1); SB const u32x4 f37 = AFR(14); const u32x4 f38 = BFR(2); e2 += dppz<0xb1>(e2); PIN(e2); SB
        acc2 = mfma(f33, f34, acc2); SB  e2 += dppz<0x4e>(e2); PIN(e2); SB
        acc1 = mfma(f33, f26, acc1); SB const u32x4 f39 = AFR(15); const u32x4 f40 = BFR(1); es = e2; XROW16(es); PIN(es); SB
        acc2 = mfma(f35, f36, acc2); SB  XROW32(es); PIN(es); SB
        acc1 = mfma(f35, f28, acc1); SB const u32x4 f41 = AFR(16); const u32x4 f42 = BFR(0); e2 = es; SB
        acc2 = mfma(f37, f38, acc2); SB const u32x4 f43 = AFR(17); const u32x4 f44 = BFR(-1); SB
        acc1 = mfma(f37, f30, acc1); SB const u32x4 f45 = AFR(18); const u32x4 f46 = BFR(-2); SB
        acc2 = mfma(f39, f40, acc2); SB const u32x4 f47 = AFR(19); const u32x4 f48 = BFR(-3); SB
        acc1 = mfma(f39, f32, acc1); SB  SB
        acc2 = mfma(f41, f42, acc2); SB  SB
        acc2 = mfma(f43, f44, acc2); SB  SB
        acc2 = mfma(f45, f46, acc2); SB  SB
        acc2 = mfma(f47, f48, acc2); SB const u32x4 f49 = AFR(20); SB
        acc1 = mfma(f41, f34, acc1); SB const u32x4 f50 = AFR(21); SB
        acc1 = mfma(f43, f36, acc1); SB const u32x4 f51 = AFR(22); fin_part<0, 3>(g2, acc2, smem, c, 2048); SB
        acc1 = mfma(f45, f38, acc1); SB const u32x4 f52 = AFR(23); fin_part<3, 6>(g2, acc2, smem, c, 2048); SB
        acc1 = mfma(f47, f40, acc1); SB  fin_part<6, 9>(g2, acc2, smem, c, 2048); SB
        acc1 = mfma(f49, f42, acc1); SB  fin_part<9, 12>(g2, acc2, smem, c, 2048); SB
        acc1 = mfma(f50, f44, acc1); SB  fin_part<12, 15>(g2, acc2, smem, c, 2048); SB
        acc1 = mfma(f51, f46, acc1); SB  fin_part<15, 18>(g2, acc2, smem, c, 2048); SB
        acc1 = mfma(f52, f48, acc1); SB  fin_part<18, 21>(g2, acc2, smem, c, 2048); fin_part<21, 24>(g2, acc2, smem, c, 2048); fin_end(g2, e2, smem, c, 512); SB
        fin_part<0, 24>(g1, acc1, smem, c, 2560);
        fin_end(g1, g2.cy, smem, c, 0);
    }
}
#undef AFR
#undef BFR
#undef EB
#undef SB
#undef XROW16
#undef XROW32

__global__ __launch_bounds__(NTHR, 3) void k_fused(const u16* __restrict__ x, const u16* __restrict__ wr, const u16* __restrict__ wi,
                                                   const u16* __restrict__ qq, const u16* __restrict__ kk, const u16* __restrict__ vv,
                                                   const u16* __restrict__ shk, const u16* __restrict__ hk, const u16* __restrict__ dd,
                                                   u16* __restrict__ out, char* ws) {
    __shared__ __attribute__((aligned(16))) char smem[LDS_BYTES];
    const int tid = threadIdx.x, lane = tid & 63;
    int w = __builtin_amdgcn_readfirstlane(tid >> 6);
    asm volatile("" : "+s"(w));
    const int n = lane & 31, hh = lane >> 5, g = lane >> 4, q = (lane >> 2) & 3, p = lane & 3;
    const unsigned lb = (unsigned)(size_t)(lds_vp)smem;
    unsigned warm = 0;
    if (w >= 9 && blockIdx.x < CODE_WGS) {
        unsigned long long pc;
        asm volatile("s_getpc_b64 %0" : "=s"(pc));
        asm volatile("global_load_dword %0, %1, %2" : "=&v"(warm) : "v"(min((tid - 576) * 128, CODE_BYTES)), "s"(pc) : "memory");
    }
    __builtin_amdgcn_s_setprio(3);
    *(unsigned*)(smem + LA_SY + (tid & 255) * 4) = 0u;
    const int i2 = blockIdx.x;
    const int xg = i2 & 7, ib = i2 >> 3;
    const int s4 = ib & 3, j = 8 * (ib >> 2) + xg, wb = w - 4;
    const int un = tid - 256, ur = (un * 3277) >> 16;
    unsigned vb = (32 * j + ur) * (D_ * 2) + 320 * s4 + (un - 20 * ur) * 16;
    unsigned va = vb - 3 * D_ * 2, v3 = un < 60 ? vb : OOB, vs = (160 * s4 + 32 * wb) * 8 + (lane & 15) * 16;
    unsigned vl = lane * 16, vt = lane < 32 ? g * (256 * D_ * 2 - 256) + vl : OOB;
    PIN(va); PIN(vb); PIN(v3); PIN(vs); PIN(vl); PIN(vt);
    asm volatile("" :: "s"(x), "s"(wr), "s"(wi), "s"(qq), "s"(kk), "s"(vv), "s"(shk), "s"(hk), "s"(dd), "s"(out), "s"(ws));
    asm volatile("s_waitcnt lgkmcnt(0)\n\ts_barrier" ::: "memory");
    const rsrc_t rs = mkrs(ws, WS_END);
    const int part = ib < 24 ? ib >> 3 : xg % 3;
    const int ch0 = 64 * (ib < 24 ? xg : min(8 + xg / 3, 9)) + 8 * (ib & 7);
    const bool hasU = i2 < 8 * 27, cons = ib < 24 || xg < 6;
    unsigned tag;
    lds_u32* const tagp = (lds_u32*)(lds_vp)(smem + LA_SY) + EV_TAG * 16;
    auto get_tag = LAM {
#pragma unroll 1
        for (unsigned spins = 0; spins < LDS_SPIN_CAP; ++spins) {
            tag = __builtin_amdgcn_readfirstlane(__hip_atomic_load(tagp, __ATOMIC_RELAXED, __HIP_MEMORY_SCOPE_WORKGROUP));
            if (tag) break;
            __builtin_amdgcn_s_sleep(1);
        }
    };
    if (w >= 9) {
        const float fn = (float)n;
#pragma unroll 1
        for (int job = w - 9; job < 5; job += 3) {
            const bool r1 = job < 3;
            const float step = r1 ? fn * (-1.0f / 40.0f) : (job == 3 ? (float)(n >> 1) * (1.0f / 16.0f) : fn * (1.0f / 640.0f));
            const float ph = (job == 3 && (n & 1)) ? (n >= 16 ? -0.25f : 0.25f) : 0.f;
            const float a0 = (float)(r1 ? ((tid >> 5) - 18) * 8 : 4 * hh) * step + ph, a1 = (r1 ? 4.f : 8.f) * step + a0;
            f32x4 fc[2], fs[2];
#pragma unroll
            for (int t = 0; t < 8; ++t) {
                const float b = (float)(t & 3) * step + (t < 4 ? a0 : a1);
                fc[t >> 2][t & 3] = fcos(b);
                fs[t >> 2][t & 3] = fsin(b);
            }
            char* dst = smem + LA_T1 + job * 2048 + lane * 16;
            if (job < 4) {
                *(u32x4*)dst = pk44(fc[0], fc[1]);
                *(u32x4*)(dst + 1024) = pk44(fs[0], fs[1]);
            } else {
                *(f32x4*)dst = fc[0]; *(f32x4*)(dst + 1024) = fc[1]; *(f32x4*)(dst + 2048) = fs[0]; *(f32x4*)(dst + 3072) = fs[1];
            }
        }
        arrive(smem, EV_T1, w, lane);
    } else if (w >= 4) {
        unsigned keep;
        asm volatile("s_mov_b32 %0, m0\n\ts_mov_b32 m0, %8\n\ts_nop 0\n\t"
                     "buffer_load_dwordx4 %1, %5, 0 offen lds\n\ts_add_u32 m0, m0, 0x1400\n\ts_nop 0\n\t"
                     "buffer_load_dwordx4 %2, %5, %10 offen lds\n\ts_mov_b32 m0, %13\n\ts_nop 0\n\t"
                     "buffer_load_dwordx4 %3, %5, %11 offen lds\n\ts_mov_b32 m0, %14\n\ts_nop 0\n\t"
                     "buffer_load_dwordx4 %4, %7, 0 offen lds\n\ts_mov_b32 m0, %9\n\ts_nop 0\n\t"
                     "buffer_load_dwordx4 %2, %6, 0 offen lds\n\ts_add_u32 m0, m0, 0x1400\n\ts_nop 0\n\t"
                     "buffer_load_dwordx4 %2, %6, %12 offen lds\n\ts_mov_b32 m0, %0"
                     : "=&s"(keep)
                     : "v"(va), "v"(vb), "v"(v3), "v"(vs), "s"(rawrs(kk, S_ * D_ * 2)), "s"(rawrs(vv, S_ * D_ * 2)), "s"(rawrs(shk, D_ * 8)),
                       "s"(lb + LA_K + wb * 1024), "s"(lb + LA_V + wb * 1024), "s"(13 * D_ * 2), "s"(29 * D_ * 2), "s"(16 * D_ * 2),
                       "s"(lb + (wb == 0 ? LA_K + 10240 : wb * CHB + LB_U)), "s"(lb + wb * CHB + LB_U + 1024)
                     : "memory", "scc");
    } else {
        const unsigned so = (512 * w + i2) * (D_ * 2);
        unsigned keep;
        asm volatile("s_mov_b32 %0, m0\n\ts_mov_b32 m0, %4\n\ts_nop 0\n\t"
                     "buffer_load_dwordx4 %1, %3, %5 offen lds\n\t"
                     "buffer_load_dwordx4 %2, %3, %5 offen offset:1024 lds\n\t"
                     "buffer_load_dwordx4 %1, %3, %6 offen offset:2048 lds\n\t"
                     "s_mov_b32 m0, %0"
                     : "=&s"(keep)
                     : "v"(vl), "v"(vt), "s"(rawrs(x, S_ * D_ * 2)), "s"(lb + LA + w * 3072), "s"(so), "s"(so + 256 * D_ * 2 - 2048)
                     : "memory");
    }
    int syo = LA_SY + (lane & 15) * 4;
    const rsrc_t ro = mkrs(out, S_ * 2 * D_ * 2);
    const int T1 = 7 - part, T2 = part == 0 ? 3 : 2 * part, T3 = 2 - part;
    if (w >= 4) {
        const int cb = wb * CHB;
        unsigned l16 = lane * 16;
        unsigned coef = (n * 0x9e3779b1u + 0x7f4a7c15u) | 0x00010001u;
        dma4(rawrs(hk, cons ? S_ * D_ * 2 : 0), l16, l16 < 1024u - 512u * part ? l16 : OOB, (ch0 + wb) * 4096, lb + cb + LB_H);
        unsigned warm2 = 0, dps;
        asm volatile("s_load_dword %0, %1, %2\n\ts_waitcnt lgkmcnt(0)" : "=&s"(dps) : "s"(dd), "s"((ch0 + (wb & 6)) * 2) : "memory");
        if (wb < 5) {
            int sto = LB_ST + wb * 2304 + (lane & 15) * 72 + g * 16;
            int sro = LB_ST + wb * 2304 + ((p & 1) * 16 + 2 * g + (p >> 1)) * 72 + 8 * (3 - q);
            int kro = LA_K + g * 2560 + wb * 64 + (lane & 15) * 4;
            unsigned mso = hh * 32768 + n * 16;
            unsigned cso = (lane & 14) ? OOB : (2 * p + hh) * 4096 + (g & 1) * 4;
            PIN(sto); PIN(sro); PIN(kro); PIN(mso); PIN(cso); PIN(coef);
            get_tag();
            VMWAIT(4);
            arrive(smem, EV_K, w, lane);
            unsigned kr[11], vr[8];
            const u32x4 sw = *(const u32x4*)(smem + cb + LB_U + 1024 + l16);
            await<1>(smem, EV_K, 0x1f0, syo, LAM {
#pragma unroll
                for (int i = 0; i < 11; ++i) kr[i] = *(const unsigned*)(smem + kro + i * 320);
#pragma unroll
                for (int i = 0; i < 8; ++i) vr[i] = *(const unsigned*)(smem + kro + (LA_V - LA_K) + i * 320);
            });
            const f32x2 wt[4] = {{bflo(sw[0]), bflo(sw[2])}, {bfhi(sw[0]), bfhi(sw[2])}, {bflo(sw[1]), bflo(sw[3])}, {bfhi(sw[1]), bfhi(sw[3])}};
            f32x2 kf[11], o[8];
#pragma unroll
            for (int i = 0; i < 11; ++i) kf[i] = f32x2{bflo(kr[i]), bfhi(kr[i])};
#pragma unroll
            for (int e = 0; e < 8; ++e) {
                f32x2 a = kf[e] * wt[3];
                a = kf[e + 1] * wt[2] + a;
                a = kf[e + 2] * wt[1] + a;
                a = kf[e + 3] * wt[0] + a;
                o[e] = a * f32x2{bflo(vr[e]), bfhi(vr[e])};
            }
#pragma unroll
            for (int c2 = 0; c2 < 2; ++c2) {
                char* dst = smem + sto + c2 * 16 * 72;
                *(u32x2*)dst = u32x2{pk(o[0][c2], o[1][c2]), pk(o[2][c2], o[3][c2])};
                *(u32x2*)(dst + 8) = u32x2{pk(o[4][c2], o[5][c2]), pk(o[6][c2], o[7][c2])};
            }
            __builtin_amdgcn_fence(__ATOMIC_RELEASE, "wavefront");
            __builtin_amdgcn_wave_barrier();
            const char* src = smem + sro;
            const u32x4 u0 = trread2(src + 32, src), u1 = trread2(src + 8 * 72 + 32, src + 8 * 72);
            const unsigned po = WS_KV + (20 * s4 + 4 * wb) * 32768 + j * 512;
            st16<16>(rs, mso, po, u0);
            st16<16>(rs, mso, po + 65536, u1);
            const unsigned h0 = rowhash(u0, coef), h1 = rowhash(u1, coef);
            st4(rs, cso, WS_CK + ((20 * s4 + 4 * wb) * 64 + j) * 64, (p ? h1 : h0) + tag);
            arrive(smem, EV_I, w, lane);
            __builtin_amdgcn_s_setprio(0);
        }
        asm volatile("" : "+s"(w));
        if (w >= 9) {
            if (wb < 7) {
                const unsigned ow = (((8 * ib + (lane >> 3)) * NW + xg * DF) * 2 + min(lane & 7, 5) * 128) & ~3u;
                __builtin_amdgcn_s_sleep(DLY_T);
                asm volatile("buffer_load_dword %0, %1, %2, 0 offen" : "=&v"(warm2) : "v"(ow), "s"(rawrs(wb == 5 ? wr : wi, S_ * DF * 2)) : "memory");
            }
            __builtin_amdgcn_s_setprio(0);
            const int e = n & 15;
            const float fd2 = (float)((e & 3) + 8 * ((e >> 2) & 1) + 4 * (e >> 3)), fh = (float)(4 * hh);
            const float step = wb == 5 ? fd2 * (1.0f / 640.0f) : (float)(((tid >> 6) - 10) * 32 + (n >> 1) + 16 * (n & 1)) * (-1.0f / 40.0f);
            f32x4 fc[3], fs[3];
#pragma unroll
            for (int t = 0; t < 12; ++t) {
                const float b = ((float)((t & 3) + 8 * (t >> 2)) + fh) * step;
                fc[t >> 2][t & 3] = fcos(b);
                fs[t >> 2][t & 3] = fsin(b);
            }
            char* dst = smem + LA_T3 + l16;
            if (wb == 5) {
#pragma unroll
                for (int t = 0; t < 3; ++t) { *(f32x4*)(dst + (8 + t) * 1024) = fc[t]; *(f32x4*)(dst + (11 + t) * 1024) = fs[t]; }
                u32x4 m0, m1;
                const float ph = n >= 16 ? 0.25f : 0.f, s16 = fd2 * (1.0f / 16.0f);
#pragma unroll
                for (int t = 0; t < 4; ++t) { m0[t] = cs(ph - ((float)t + fh) * s16); m1[t] = cs(((float)(8 + t) + fh) * s16 - ph); }
                *(u32x4*)dst = m0; *(u32x4*)(dst + 1024) = m1;
            } else {
                constexpr float W2 = 2.0f / (2048.0f * 640.0f);
                const f32x4 wa = {hh ? W2 : 0.5f * W2, W2, W2, W2}, wc = {hh ? 0.5f * W2 : W2, hh ? 0.f : W2, hh ? 0.f : W2, hh ? 0.f : W2};
                dst += (2 + 3 * (wb - 6)) * 1024;
                *(u32x4*)dst = pk44(fc[0] * wa, fc[1] * W2);
                *(u32x4*)(dst + 1024) = pk44(fs[0] * wa, fs[1] * W2);
                *(u32x4*)(dst + 2048) = pk44(fc[2] * wc, fs[2] * wc);
            }
            arrive(smem, EV_T3, w, lane);
            VMWAIT(0);
            asm volatile("" :: "v"(warm), "v"(warm2));
        }
        if (cons) {
            ConvCtx c;
            {
                if (lane < 36) *(u32x4*)(smem + cb + l16 + (lane < 4 ? 0 : LB_Z - 64)) = u32x4{0u, 0u, 0u, 0u};
                const int pi = 2 * (g & 1) + (p >> 1) + 4 * (p & 1);
                const int a = p | ((g & 1) << 2), am = (a + 7) & 7;
                c.pa = cb + 8 + 8 * (8 * hh + q + pi);
                c.pb = cb + LB_U - 384 + 64 * (a - hh) + 16 * q;
                c.bz = cb + LB_U - 64 + 16 * q - 64 * (4 * g + p);
                c.ba = cb + LB_H + 8 * (32 * g + 8 * p + q);
                c.pu = c.pb + 392 + 56 * hh;
                c.csrc = 4 * ((hh ? n : ((am & 3) | (q << 2) | ((am >> 2) << 4))) + 32 - 32 * hh);
                c.yo = cb + LB_Y + lane * 8;
                c.first = (lane & 0x33) == 0;
                c.diag = g == p;
                c.p0 = p == 0;
                c.dv = (w & 1) ? bfhi(dps) : bflo(dps);
                PIN(c.pa); PIN(c.pb); PIN(c.bz); PIN(c.ba); PIN(c.pu); PIN(c.csrc); PIN(c.yo);
            }
            unsigned oro = (32 * q + g + 4 * p + 128 * (wb & 1) + 16 * ((wb >> 1) & 1)) * (4 * D_);
            int yso = q * CHB + LB_Y + wb * 128 + g * 32 + p * 8;
            const int nk = (32 - 4 * part - wb + 7) >> 3;
            const unsigned m3 = (nk - 1) * 8;
            const unsigned mo = WS_KV + (ch0 >> 3) * 32768 + wb * 1024, cko = WS_CK + ((ch0 >> 3) * 64 + 2 * wb) * 64;
            int uo = ((lane >> 2) & 7) * CHB + LB_U + wb * 128 + hh * 64 + p * 16;
            unsigned fo = (min(p, nk - 1) * 16 + hh) * 64 + (g & 1) * 4;
            PIN(oro); PIN(yso); PIN(syo); PIN(l16); PIN(uo); PIN(fo); PIN(coef);
            __builtin_amdgcn_s_setprio(1);
            get_tag();
            await<1>(smem, EV_S, 0xf, syo, LAM {});
#pragma unroll 1
            for (unsigned spins = 0; spins < SPIN_CAP; ++spins) {
                asm volatile("" ::: "memory");
                u32x4 um[4];
                unsigned hs[4];
                const unsigned ck = ld4(rs, fo, cko);
#pragma unroll
                for (int k = 0; k < 4; ++k) um[k] = ld16(rs, l16, mo + (k < 3 ? 8 * k : m3) * 1024);
#pragma unroll
                for (int k = 0; k < 4; ++k) {
                    *(u32x4*)(smem + uo + (k < 3 ? 1024 * k : 128 * m3)) = um[k];
                    hs[k] = rowhash(um[k], coef);
                }
                const unsigned ha = c.p0 ? hs[0] : hs[1], hb = p == 2 ? hs[2] : hs[3];
                if (__builtin_amdgcn_ballot_w64((p < 2 ? ha : hb) + tag != ck) == 0) break;
                __builtin_amdgcn_s_sleep(2);
            }
            VMWAIT(0);
            arrive(smem, EV_U, w, lane);
            await<1>(smem, EV_U, 0xff0, syo, LAM {});
            auto pause = LAM { if (hasU) await<1>(smem, EV_P2B, 0xf, syo, LAM {}); };
            if (part == 0) conv<0>(smem, c, pause);
            else if (part == 1) conv<1>(smem, c, pause);
            else conv<2>(smem, c, pause);
            arrive(smem, EV_Y, w, lane);
            const char* ys = smem + yso;
            const char* qs = smem + LA_Q + wb * 1024 + l16;
            const char* qs1 = qs + (wb < 4 ? 8192 : 0);
            u32x4 y[2], qv[2];
            await<3>(smem, EV_Y, 0xfff, syo, LAM {
                qv[0] = *(const u32x4*)qs; qv[1] = *(const u32x4*)qs1;
                y[0] = trread2(ys, ys + 4 * CHB);
                y[1] = trread2(ys + (LB_U + 3584 - LB_Y), ys + (LB_U + 3584 - LB_Y) + 4 * CHB);
            });
#pragma unroll
            for (int k = 0; k < 2; ++k) {
                if (k == 1 && !(part && wb < 4)) break;
                u32x4 o;
#pragma unroll
                for (int e = 0; e < 4; ++e) o[e] = pk(bflo(y[k][e]) * bflo(qv[k][e]), bfhi(y[k][e]) * bfhi(qv[k][e]));
                st16<0>(ro, oro, (D_ + ch0) * 2 + (k == 0 ? (wb < 4 ? T1 : T2) : T3) * (256 * D_ * 4), o);
            }
        }
    } else {
        unsigned ep = ld4(rs, WS_EP, 0);
        __builtin_amdgcn_s_setprio(2);
        const int pi1 = min(28 * w + n, PK - 1);
        const int pi2 = 4 * ib + p;
        const unsigned we = (4 * w + q + 64 * g) * NW + xg * DF + 3 * pi2;
        unsigned vo = (we * 2) & ~3u;
        int l16 = lane * 16 + LA_T1;
        int xao = LA + w * 3072 + (g & 1) * 2048 + (8 * hh + q) * 32 + p * 8;
        int xto = LA + w * 3072 + 1024 + g * 256 + q * 32 + p * 8;
        int exw = LA_EX + (42 * hh + n) * 48 + 8 * w;
        u32x4 c1m[2];
#pragma unroll
        for (int t = 0; t < 4; ++t) {
            const unsigned f = cs((float)((256 * (4 * hh + t) + i2) * (n >> 2)) * (1.0f / 2048.0f) + 0.25f * (n & 1));
            c1m[0][t] = (n & 2) ? 0u : f;
            c1m[1][t] = (n & 2) ? f : 0u;
        }
        int exr[3];
#pragma unroll
        for (int e = 0; e < 3; ++e) {
            const int kd = min(3 * pi1 + e, 320);
            const int m = (kd * 1639) >> 16, cc = kd - 40 * m;
            const int sa = 21 * m + cc, sb = 355 - sa;
            exr[e] = LA_EX + (cc <= 20 ? sa : sb) * 48 + 16 * hh;
            PIN(exr[e]);
        }
        unsigned x1o = (n < 28 && 28 * w + n < PK) ? WS_X1 + (i2 >> 1) * (2 * ROWB) + (i2 & 1) * 64 + (pi1 >> 2) * 128 + (pi1 & 3) * 16 + hh * (PK * 32) : OOB;
        PIN(c1m[0]); PIN(c1m[1]); PIN(xao); PIN(xto); PIN(exw); PIN(x1o); PIN(l16); PIN(vo); PIN(syo);
        {
            u32x4 tb[12];
            await<1>(smem, EV_T1, 0xe00, syo, LAM {
#pragma unroll
                for (int f = 0; f < 12; ++f) tb[f] = *(const u32x4*)(smem + l16 + f * 1024);
            });
            FENCE();
            VMWAIT(0);
            const char* xa = smem + xao;
            const char* xt = smem + xto;
            const u32x4 a0 = trread2(xa, xa + 128), a1 = trread2(xa + 512, xa + 640), a2 = trread2(xt, xt + 128);
            FENCE();
            tag = 2u * __builtin_amdgcn_readfirstlane(ep) + 3u;
            if (tid == 0) __hip_atomic_store(tagp, tag, __ATOMIC_RELAXED, __HIP_MEMORY_SCOPE_WORKGROUP);
            FENCE();
            f32x16 tr = mfma(a0, tb[0], zero16());
            f32x16 ti = mfma(a0, tb[1], zero16());
            tr = mfma(a1, tb[2], tr);
            ti = mfma(a1, tb[3], ti);
            tr = mfma(a2, tb[4], tr);
            ti = mfma(a2, tb[5], ti);
            f32x16 y[2];
#pragma unroll
            for (int ss = 0; ss < 2; ++ss) {
                const f32x4 r0 = sub4(tr, 8 * ss), r1 = sub4(tr, 8 * ss + 4), i0 = sub4(ti, 8 * ss), i1 = sub4(ti, 8 * ss + 4);
                f32x4 m2 = r0 * asf(tb[10]), m3 = r1 * asf(tb[11]), m0 = i0 * asf(tb[10]), m1 = i1 * asf(tb[11]);
                FENCE();
                m0 = r0 * asf(tb[8]) + m0; m1 = r1 * asf(tb[9]) + m1;
                m2 = i0 * asf(tb[8]) - m2; m3 = i1 * asf(tb[9]) - m3;
                FENCE();
                const u32x4 pr = pk44(m0, m1), pi_ = pk44(m2, m3);
                FENCE();
                y[ss] = mfma(tb[6], pr, zero16());
                y[ss] = mfma(tb[7], pi_, y[ss]);
            }
            if (n <= 20) {
#pragma unroll
                for (int t = 0; t < 8; ++t)
                    *(u32x2*)(smem + exw + 21 * ((t & 1) + 4 * (t >> 1)) * 48) = u32x2{pk(y[0][2 * t], y[0][2 * t + 1]), pk(y[1][2 * t], y[1][2 * t + 1])};
            }
            arrive(smem, EV_X, w, lane);
            u32x4 b0, b1, b2;
            await<0, true>(smem, EV_X, 0xf, syo, LAM { b0 = *(const u32x4*)(smem + exr[0]); b2 = *(const u32x4*)(smem + exr[2]); b1 = *(const u32x4*)(smem + exr[1]); });
            f32x16 o0 = mfma(c1m[0], b0, zero16());
            f32x16 o1 = mfma(c1m[0], b2, zero16());
            o0 = mfma(c1m[1], b1, o0);
            u32x4 pc[4];
#pragma unroll
            for (int gq = 0; gq < 4; ++gq)
                pc[gq] = piece(pk(o0[4 * gq], o0[4 * gq + 1]), pk(o0[4 * gq + 2], o0[4 * gq + 3]), pk(o1[4 * gq], o1[4 * gq + 1]), tag);
#pragma unroll
            for (int gq = 0; gq < 4; ++gq) st16<16>(rs, x1o, gq * 2 * PK * 32, pc[gq]);
        }
        arrive(smem, EV_I, w, lane);
        __builtin_amdgcn_s_setprio(0);
        u32x4 ci[2];
        unsigned zoff = WS_Z1 + i2 * ROWB + pi1 * 16 + 4 * hh * (PK * 16);
        unsigned qro = (32 * q + g + 4 * p + 128 * (w & 1) + 16 * (w >> 1)) * (D_ * 2);
        {
            unsigned coloff = (64 * g + 4 * w + q) * ROWB + (xg * PK + pi2) * 16;
            int ewo = LA + (g * 768 + (16 * w + p) * 4 + q) * 4, ero = LA + w * 3072 + lane * 16;
            unsigned xoff = (hasU ? WS_X1 : OOB) + (64 * g + 4 * w + (q & 2)) * ROWB + (xg * 27 + ib) * 128 + (lane & 7) * 16;
            PIN(coloff); PIN(ewo); PIN(ero); PIN(xoff); PIN(zoff); PIN(qro);
            unsigned* ew = (unsigned*)(smem + ewo);
            const char* er = smem + ero;
            u32x4 fre, fim, fin;
            f32x4 tc, ts;
#pragma unroll
            for (int t = 0; t < 4; ++t) {
                const float an = (float)((lane & 15) * (4 * g + t)) * (1.0f / 16.0f);
                const float c = fcos(an), s = fsin(an);
                fre[t] = pk(c, s);
                fim[t] = pk(-s, c);
                fin[t] = fim[t] ^ 0x80008000u;
                const float b = (float)((4 * w + q) * (4 * g + t)) * (1.0f / 256.0f);
                tc[t] = fcos(b); ts[t] = fsin(b);
                const unsigned f = cs(0.25f * (n & 1) - (float)((256 * (n >> 2) + i2) * (4 * hh + t)) * (1.0f / 2048.0f));
                ci[0][t] = (n & 2) ? 0u : f;
                ci[1][t] = (n & 2) ? f : 0u;
            }
            PIN(fre); PIN(fim); PIN(fin); PIN(tc); PIN(ts); PIN(ci[0]); PIN(ci[1]);
            __builtin_amdgcn_s_setprio(2);
            await<1>(smem, EV_I, 0x1ff, syo, LAM {});
            u32x2 wraw[4][2];
            {
                const rsrc_t rwr = mkrs(wr, hasU ? S_ * DF * 2 : 0), rwi = mkrs(wi, hasU ? S_ * DF * 2 : 0);
#pragma unroll
                for (int r = 0; r < 4; ++r) {
                    wraw[r][0] = __builtin_amdgcn_raw_buffer_load_b64(rwr, vo, r * 16 * NW * 2, 0);
                    wraw[r][1] = __builtin_amdgcn_raw_buffer_load_b64(rwi, vo, r * 16 * NW * 2, 0);
                }
                FENCE();
            }
            __builtin_amdgcn_s_sleep(PRE_A);
            u32x4 xr[4], f[3];
            issue4(rs, xoff, 16 * ROWB, xr);
            arrive(smem, EV_S, w, lane);
            FENCE();
            f32x4 wf[2][3];
            const unsigned sh = (we & 1) * 16;
#pragma unroll
            for (int r = 0; r < 4; ++r)
#pragma unroll
                for (int a = 0; a < 2; ++a) {
                    const unsigned lo = __builtin_amdgcn_alignbit(wraw[r][a][1], wraw[r][a][0], sh), hi = wraw[r][a][1] >> sh;
                    wf[a][0][r] = bflo(lo); wf[a][1][r] = bfhi(lo); wf[a][2][r] = bflo(hi);
                }
#pragma unroll
            for (int e = 0; e < 3; ++e) { PIN(wf[0][e]); PIN(wf[1][e]); }
            FENCE();
            settle4(rs, xoff, 16 * ROWB, hasU ? tag : 0u, xr, f);
            if (hasU) {
                f32x4 re[3], im[3], mr[3], mi[3];
#define M16(e, FI) re[e] = mfma16(fre, f[e], zero4()); im[e] = mfma16(FI, f[e], zero4()); FENCE();
#define CMUL(e, C, S) mi[e] = re[e] * S; mr[e] = re[e] * C; mr[e] = im[e] * S + mr[e]; mi[e] = im[e] * C - mi[e]; f[e] = pkc(mr[e], mi[e]);
#define TW(e, OFF) CMUL(e, tc, ts) _Pragma("unroll") for (int r = 0; r < 4; ++r) ew[OFF + e * 256 + r * 16] = f[e][r]; FENCE();
#define RD3(OFF) LAM { f[0] = *(const u32x4*)(er + (OFF)); f[1] = *(const u32x4*)(er + (OFF) + 1024); f[2] = *(const u32x4*)(er + (OFF) + 2048); }
                M16(0, fim) M16(1, fim) M16(2, fim)
                TW(0, (LA_E1 - LA) / 4) TW(1, (LA_E1 - LA) / 4) TW(2, (LA_E1 - LA) / 4)
                arrive(smem, EV_P2A, w, lane);
                await<0, true>(smem, EV_P2A, 0xf, syo, RD3(LA_E1 - LA));
                M16(0, fin) M16(1, fin)
                CMUL(0, wf[0][0], wf[1][0]) FENCE();
                M16(2, fin) M16(0, fim)
                CMUL(1, wf[0][1], wf[1][1]) FENCE();
                M16(1, fim)
                CMUL(2, wf[0][2], wf[1][2]) FENCE();
                M16(2, fim)
                TW(0, 0) TW(1, 0) TW(2, 0)
                arrive(smem, EV_P2B, w, lane);
                await<0, true>(smem, EV_P2B, 0xf, syo, RD3(0));
                M16(0, fin) M16(1, fin)
                f[0] = pkc(re[0], im[0]); FENCE();
                M16(2, fin)
                f[1] = pkc(re[1], im[1]); FENCE();
                f[2] = pkc(re[2], im[2]);
#pragma unroll
                for (int r = 0; r < 4; ++r) st16<16>(rs, coloff, WS_Z1 + r * 16 * ROWB, piece(f[0][r], f[1][r], f[2][r], tag));
            }
        }
        if (cons) await<1>(smem, EV_U, 0xff0, syo, LAM {});
        {
            const i32x4 rq = rawrs(qq, cons ? S_ * D_ * 2 : 0);
            dma1(rq, qro, ch0 * 2 + T1 * (256 * D_ * 2), lb + LA_Q + w * 1024);
            dma1(rq, qro, ch0 * 2 + T2 * (256 * D_ * 2), lb + LA_Q + 4096 + w * 1024);
            dma1(rq, qro, ch0 * 2 + (part ? T3 : 64) * (256 * D_ * 2), lb + LA_Q + 8192 + w * 1024);
        }
        __builtin_amdgcn_s_setprio(0);
        const int k1c = min(n, 20);
        int e3o = LA_E3 + (hh * 324 + 3 * pi1) * 4, s0o = LA_E3 + (2 * w * 324 + 160 * hh + k1c) * 4, s1o = LA_E3 + (2 * w * 324 + 200 - 160 * hh - k1c) * 4;
        unsigned oo = (256 * (2 * w + hh) + i2) * (4 * D_) + 16 * n;
        unsigned oo2 = n < 16 ? oo + 1024 : OOB;
        PIN(e3o); PIN(s0o); PIN(s1o); PIN(oo); PIN(oo2);
        u32x4 tc[14];
        await<1>(smem, EV_T3, 0xe00, syo, LAM {
#pragma unroll
            for (int f = 0; f < 14; ++f) tc[f] = *(const u32x4*)(smem + LA_T3 + lane * 16 + f * 1024);
        });
#pragma unroll
        for (int f = 0; f < 14; ++f) PIN(tc[f]);
        VMWAIT(3);
        if (!hasU) {
#pragma unroll 1
            for (unsigned spins = 0; spins < SPIN_CAP; ++spins) {
                asm volatile("" ::: "memory");
                const u32x4 v = ld16(rs, WS_Z1 + i2 * ROWB + (w * 2 * PK + (lane & 7) * 27) * 16, 0);
                if (__builtin_amdgcn_ballot_w64(xor3(xor3(v[0], v[1], v[2]), v[3], tag) != 0) == 0) break;
                __builtin_amdgcn_s_sleep(3);
            }
        }
        {
            u32x4 zr[4], zb[3];
            __builtin_amdgcn_s_sleep(PRE_Z);
            asm volatile("" ::: "memory");
            issue4(rs, zoff, PK * 16, zr);
            VMWAIT(4);
            arrive(smem, EV_Y, w, lane);
            settle4(rs, zoff, PK * 16, tag, zr, zb);
            f32x16 o0 = mfma(ci[0], zb[0], zero16());
            f32x16 o1 = mfma(ci[0], zb[2], zero16());
            o0 = mfma(ci[1], zb[1], o0);
            unsigned* e3 = (unsigned*)(smem + e3o);
#pragma unroll
            for (int gq = 0; gq < 4; ++gq) {
                e3[gq * 648] = pk(o0[4 * gq], o0[4 * gq + 1]);
                e3[gq * 648 + 1] = pk(o0[4 * gq + 2], o0[4 * gq + 3]);
                e3[gq * 648 + 2] = pk(o1[4 * gq], o1[4 * gq + 1]);
            }
        }
        arrive(smem, EV_P3, w, lane);
        const unsigned* s0 = (const unsigned*)(smem + s0o);
        const unsigned* s1 = (const unsigned*)(smem + s1o);
        u32x4 f0, f1, f2, f3;
        await<0, true>(smem, EV_P3, 0xf, syo, LAM {
            f0 = u32x4{s0[0], s0[40], s0[80], s0[120]}; f1 = u32x4{s0[324], s0[364], s0[404], s0[444]};
            f2 = u32x4{s1[120], s1[80], s1[40], s1[0]}; f3 = u32x4{s1[444], s1[404], s1[364], s1[324]};
        });
        f32x16 tr = mfma(f0, tc[0], zero16());
        f32x16 ti = mfma(f1, tc[0], zero16());
        tr = mfma(f2, tc[1], tr);
        ti = mfma(f3, tc[1], ti);
        f32x4 re[3], im[3], mr[3], mi[3];
#pragma unroll
        for (int r = 0; r < 12; ++r) {
            auto sw = __builtin_amdgcn_permlane16_swap(__float_as_uint(tr[r]), __float_as_uint(ti[r]), false, false);
            re[r >> 2][r & 3] = __uint_as_float(sw[0]);
            im[r >> 2][r & 3] = __uint_as_float(sw[1]);
        }
        FENCE();
#pragma unroll
        for (int t = 0; t < 3; ++t) { mr[t] = im[t] * asf(tc[11 + t]); mi[t] = re[t] * asf(tc[11 + t]); }
        FENCE();
#pragma unroll
        for (int t = 0; t < 3; ++t) { mr[t] = re[t] * asf(tc[8 + t]) - mr[t]; mi[t] = im[t] * asf(tc[8 + t]) + mi[t]; }
        FENCE();
        const u32x4 a0 = pk44(mr[0], mr[1]), a1 = pk44(mi[0], mi[1]), a2 = pk44(mr[2], mi[2]);
        f32x16 y0 = mfma(a0, tc[2], zero16());
        y0 = mfma(a1, tc[3], y0);
        y0 = mfma(a2, tc[4], y0);
        f32x16 y1 = mfma(a0, tc[5], zero16());
        y1 = mfma(a1, tc[6], y1);
        y1 = mfma(a2, tc[7], y1);
        u32x4 o1, o2, o3;
        {
            const u32x4 xa = pack8(y0, 0), xb = pack8(y0, 8);
#pragma unroll
            for (int e = 0; e < 4; ++e) {
                auto sw = __builtin_amdgcn_permlane32_swap(xa[e], xb[e], false, false);
                const unsigned lo = sw[0], hi = sw[1], lo2 = dppu<0xb1>(lo), hi2 = dppu<0xb1>(hi);
                o1[e] = (n & 1) ? hi2 : lo;
                o2[e] = (n & 1) ? hi : lo2;
            }
        }
        st16<2>(ro, oo, 0, o1);
        st16<2>(ro, oo + 512, 0, o2);
        {
            const u32x4 xa = pack8(y1, 0), xb = pack8(y1, 8);
#pragma unroll
            for (int e = 0; e < 4; ++e) {
                auto sw = __builtin_amdgcn_permlane32_swap(xa[e], xb[e], false, false);
                const unsigned lo = sw[0], hi2 = dppu<0xb1>(sw[1]);
                o3[e] = (n & 1) ? hi2 : lo;
            }
        }
        st16<2>(ro, oo2, 0, o3);
        if (i2 == 0 && tid == 0) st4(rs, WS_EP, 0, ep + 1u);
    }
}

extern "C" void kernel_launch(void* const* d_in, const int* in_sizes, int n_in, void* d_out, int out_size, void* d_ws,
                              size_t ws_size, hipStream_t stream) {
    k_fused<<<256, NTHR, 0, stream>>>((const u16*)d_in[0], (const u16*)d_in[1], (const u16*)d_in[2], (const u16*)d_in[3],
                                      (const u16*)d_in[4], (const u16*)d_in[5], (const u16*)d_in[6], (const u16*)d_in[7],
                                      (const u16*)d_in[8], (u16*)d_out, (char*)d_ws);
}
```
